```python
import torch

def _softmax(x, axis=-1):
    return torch.softmax(x, dim=axis)

def _gelu(x):
    return torch.nn.functional.gelu(x, approximate='tanh')

def _silu(x):
    return torch.nn.functional.silu(x)

def _sigmoid(x):
    return torch.sigmoid(x)

def _layernorm(x, axis=-1, eps=1e-05):
    m = x.mean(dim=axis, keepdim=True)
    v = x.var(dim=axis, keepdim=True, unbiased=False)
    return (x - m) / torch.sqrt(v + eps)

def _scatter_add(tgt, idx, src, axis=0):
    return tgt.clone().index_add_(axis, idx, src)

def _segment_sum(src, seg, n):
    out = torch.zeros((n,) + src.shape[1:], dtype=src.dtype)
    return out.index_add_(0, seg, src)

def _pad(x, pw):
    flat = tuple((v for pair in reversed(pw) for v in pair))
    return torch.nn.functional.pad(x, flat)
kernel_name = 'adaLN_cond__memory_aug_recurrent_mem_tr__swiglu_ffn__prompt'

def setup_inputs():

    _w = lambda a: (lambda t: t.to(torch.bfloat16) if t.is_floating_point() else t)(torch.as_tensor(a))

    def _A_setup():
        B, S, Dm = (1, 2048, 512)
        H, Dh = (32, 128)
        M, seg = (8, 32)
        seg = min(seg, S)
        S = S // seg * seg
        return {'x': _w((lambda *s: torch.randn(*s, dtype=torch.bfloat16) * 0.05)(B, S, Dm)), 'mem0': _w((lambda *s: torch.randn(*s, dtype=torch.bfloat16) * 0.4)(B, M, Dm)), 'Wq': _w((lambda *s: torch.randn(*s, dtype=torch.bfloat16) * 3.2)(Dm, H, Dh) * Dm ** (-0.5)), 'Wk': _w((lambda *s: torch.randn(*s, dtype=torch.bfloat16) * 3.2)(Dm, H, Dh) * Dm ** (-0.5)), 'Wv': _w((lambda *s: torch.randn(*s, dtype=torch.bfloat16) * 0.4)(Dm, H, Dh) * Dm ** (-0.5)), 'Wo': _w((lambda *s: torch.randn(*s, dtype=torch.bfloat16) * 0.4)(H, Dh, Dm) * (H * Dh) ** (-0.5)), 'seg_len': seg}
    _A_in = _A_setup()

    def _B_setup():
        B, S, D, F = (1, 2048, 512, 1792)
        return {'x': _w((lambda *s: torch.randn(*s, dtype=torch.bfloat16) * 0.05)(B, S, D)), 'w_gate': _w((lambda *s: torch.randn(*s, dtype=torch.bfloat16) * 0.05)(D, F)), 'w_up': _w((lambda *s: torch.randn(*s, dtype=torch.bfloat16) * 0.05)(D, F)), 'w_down': _w((lambda *s: torch.randn(*s, dtype=torch.bfloat16) * 0.05)(F, D))}
    _B_in = _B_setup()
    return {'A_x': _A_in['x'], 'A_mem0': _A_in['mem0'], 'A_Wq': _A_in['Wq'], 'A_Wk': _A_in['Wk'], 'A_Wv': _A_in['Wv'], 'A_Wo': _A_in['Wo'], 'A_seg_len': _A_in['seg_len'], 'B_x': _B_in['x'], 'B_w_gate': _B_in['w_gate'], 'B_w_up': _B_in['w_up'], 'B_w_down': _B_in['w_down'], 'c': _w((lambda *s: torch.randn(*s, dtype=torch.bfloat16) * 0.1)(1, 512) * 8.0), 'w_mod': _w((lambda *s: torch.randn(*s, dtype=torch.bfloat16) * 0.1)(512, 6 * 512) * 512 ** (-0.5) * 8.0)}

def reference(A_x, A_mem0, A_Wq, A_Wk, A_Wv, A_Wo, A_seg_len, B_x, B_w_gate, B_w_up, B_w_down, c, w_mod):

    def _A_ref(x, mem0, Wq, Wk, Wv, Wo, seg_len):
        B, S, Dm = x.shape
        H, Dh = (Wq.shape[1], Wq.shape[2])
        M = mem0.shape[1]
        scale = Dh ** (-0.5)
        n_seg = S // seg_len
        L = M + seg_len
        idx = torch.arange(L)
        mask = (idx[:, None] < M) | (idx[:, None] >= idx[None, :])
        mem = mem0
        outs = []
        for s in range(n_seg):
            xs = x[:, s * seg_len:(s + 1) * seg_len, :].clone()
            h = torch.concatenate([mem, xs], axis=1)
            q = torch.einsum('btd,dhe->bhte', h, Wq)
            k = torch.einsum('btd,dhe->bhte', h, Wk)
            v = torch.einsum('btd,dhe->bhte', h, Wv)
            scores = torch.einsum('bhqe,bhke->bhqk', q, k) * scale
            scores = torch.where(mask[None, None], scores, -1000000000.0)
            p = _softmax(scores, axis=-1)
            o = torch.einsum('bhqk,bhke->bhqe', p, v)
            o = torch.einsum('bhte,hed->btd', o, Wo)
            mem = o[:, :M, :].clone()
            outs.append(o[:, M:, :])
        return torch.concatenate(outs, axis=1)

    def _B_ref(x, w_gate, w_up, w_down):
        g = _silu(x @ w_gate)
        u = x @ w_up
        return g * u @ w_down
    x = A_x
    mod = (_silu(c) @ w_mod).reshape(c.shape[0], 6, 1, -1)
    sa, ca, ga, sb, cb, gb = (mod[:, 0].clone(), mod[:, 1].clone(), mod[:, 2].clone(), mod[:, 3].clone(), mod[:, 4].clone(), mod[:, 5].clone())
    h = _layernorm(x, axis=-1) * (1 + ca) + sa
    x = x + ga * _A_ref(h, A_mem0, A_Wq, A_Wk, A_Wv, A_Wo, A_seg_len)
    h = _layernorm(x, axis=-1) * (1 + cb) + sb
    x = x + gb * _B_ref(h, B_w_gate, B_w_up, B_w_down)
    return x
```

```cpp
#include <hip/hip_runtime.h>
#include <hip/hip_bf16.h>
#include <stdint.h>
#include <type_traits>
#include <utility>
#define GDELAY 1300

namespace {
typedef unsigned short u16;
typedef unsigned int u32;
typedef unsigned long long u64;
typedef __bf16 bf16x2 __attribute__((ext_vector_type(2)));
typedef __bf16 bf16x4 __attribute__((ext_vector_type(4)));
typedef __bf16 bf16x8 __attribute__((ext_vector_type(8)));
typedef __bf16 bf16x16 __attribute__((ext_vector_type(16)));
typedef short s16x4 __attribute__((ext_vector_type(4)));
typedef short s16x8 __attribute__((ext_vector_type(8)));
typedef u16 u16x2 __attribute__((ext_vector_type(2)));
typedef float f32x2 __attribute__((ext_vector_type(2)));
typedef float f32x4 __attribute__((ext_vector_type(4)));
typedef u32 u32x2 __attribute__((ext_vector_type(2)));
typedef u32 u32x4 __attribute__((ext_vector_type(4)));
typedef u32 u32x8 __attribute__((ext_vector_type(8)));
#define GAS __attribute__((address_space(1)))
#define LAS __attribute__((address_space(3)))
#define DI __device__ __forceinline__
#define RLX_AGENT __ATOMIC_RELAXED, __HIP_MEMORY_SCOPE_AGENT
typedef LAS unsigned char* ldsp;

#define ST_AUX 16
#define LD_AUX 16
#define LD_SC " sc1"
constexpr float SM_SCALE = 0.08838834764831845f, LOG2E = 1.4426950408889634f;
constexpr int DM = 512, NH = 32, NSEG = 64, FF = 1792;
constexpr size_t MB = 1u << 20;
constexpr size_t OFF_MOD = 0, OFF_LN2 = 16384  , OFF_CTL = 65536, OFF_ACC = 1 * MB, OFF_H1F = 4 * MB, OFF_X2 = 6 * MB, OFF_H2F = 8 * MB, OFF_PXF = 10 * MB,
                 OFF_PMF = 14 * MB, OFF_TF = 16 * MB, OFF_QF = 24 * MB, OFF_KF = 40 * MB, OFF_ZF = 56 * MB, OFF_UF = 72 * MB, WS_END = 136 * MB;
constexpr u32 CTL_BMX = 0;
constexpr u32 CTL_TMO = 1024;
constexpr u32 CTL_PROG = 4096;
constexpr u32 CTL_FIN = 2048;
constexpr u32 CTL_FQK = 8192;
constexpr u32 CTL_CPY = 16384;
constexpr u32 CTL_H2 = 24576;
constexpr u32 CTL_CT = 32768;
constexpr u32 SPIN_CAP = 1u << 17;
constexpr int L_CTL = 163776;
constexpr int PY_SPLIT = 40;

struct Params {
    const u16 *x, *mem0, *wq, *wk, *wv, *wo, *wg, *wu, *wd, *c, *wmod;
    u16* out;
    unsigned char* ws;
};

typedef const __attribute__((address_space(4))) Params* KP;
#define FRESH(p) asm volatile("" : "+s"(p))
template <class F, int... I> DI void sfor_(F&& f, std::integer_sequence<int, I...>) { (f(std::integral_constant<int, I>{}), ...); }
template <int N, class F> DI void sfor(F&& f) { sfor_(f, std::make_integer_sequence<int, N>{}); }
DI float lo_f(u32 v) { return __builtin_bit_cast(float, v << 16); }
DI float hi_f(u32 v) { return __builtin_bit_cast(float, v & 0xffff0000u); }
DI u32 pk(float a, float b) { f32x2 f = {a, b}; return __builtin_bit_cast(u32, __builtin_convertvector(f, bf16x2)); }
DI float rbf(float a) { return lo_f(pk(a, a)); }
DI bf16x8 as_bf(u32x4 v) { return __builtin_bit_cast(bf16x8, v); }
DI f32x4 mfma32(bf16x8 a, bf16x8 b, f32x4 c) { return __builtin_amdgcn_mfma_f32_16x16x32_bf16(a, b, c, 0, 0, 0); }
DI f32x4 mfma16(u32x2 a, u32x2 b, f32x4 c) { return __builtin_amdgcn_mfma_f32_16x16x16bf16_1k(__builtin_bit_cast(s16x4, a), __builtin_bit_cast(s16x4, b), c, 0, 0, 0); }
DI f32x4 smfma(bf16x8 a, bf16x16 b, f32x4 c, int idx) { return __builtin_amdgcn_smfmac_f32_16x16x64_bf16(a, b, c, idx, 0, 0); }
#define SMF_A(acc, a, b) asm volatile("v_smfmac_f32_16x16x64_bf16 %0, %1, %2, %3" : "+v"(acc) : "v"(a), "a"(b), "v"(idx))
#define SMF_V(acc, a, b) asm volatile("v_smfmac_f32_16x16x64_bf16 %0, %1, %2, %3" : "+v"(acc) : "v"(a), "v"(b), "v"(idx))
#define SMF_AM(acc, a, b, m, x) do { u32 t_; asm volatile("v_smfmac_f32_16x16x64_bf16 %0, %3, %4, %5\n\tv_and_b32 %2, 0x7fff7fff, %6\n\tv_pk_max_u16 %1, %1, %2" : "+v"(acc), "+v"(m), "=&v"(t_) : "v"(a), "a"(b), "v"(idx), "v"(x)); } while (0)
#define MF32(acc, a, b) asm volatile("v_mfma_f32_16x16x32_bf16 %0, %1, %2, %0" : "+v"(acc) : "v"(a), "v"(b))
#define MF32Z(acc, a, b) asm volatile("v_mfma_f32_16x16x32_bf16 %0, %1, %2, 0" : "=&v"(acc) : "v"(a), "v"(b))
#define MF32ZN(acc, a, b) asm volatile("s_nop 1\n\tv_mfma_f32_16x16x32_bf16 %0, %1, %2, 0" : "=&v"(acc) : "v"(a), "v"(b))
#define MF16N(acc, a, b) asm volatile("s_nop 1\n\tv_mfma_f32_16x16x16_bf16 %0, %1, %2, %0" : "+v"(acc) : "v"(a), "v"(b))
DI float vmax(float a, float b) { float r; asm("v_max_f32 %0, %1, %2" : "=v"(r) : "v"(a), "v"(b)); return r; }
DI float vmax3(float a, float b, float c) { float r; asm("v_max3_f32 %0, %1, %2, %3" : "=v"(r) : "v"(a), "v"(b), "v"(c)); return r; }
#define SMF4(q0, k0, q1, k1, a, wq0, wk0, wq1, wk1) asm volatile( \
    "v_smfmac_f32_16x16x64_bf16 %0, %4, %5, %9\n\tv_smfmac_f32_16x16x64_bf16 %1, %4, %6, %9\n\t" \
    "v_smfmac_f32_16x16x64_bf16 %2, %4, %7, %9\n\tv_smfmac_f32_16x16x64_bf16 %3, %4, %8, %9" \
    : "+v"(q0), "+v"(k0), "+v"(q1), "+v"(k1) : "v"(a), "a"(wq0), "a"(wk0), "a"(wq1), "a"(wk1), "v"(idx))
#define SMF4_M(q0, k0, q1, k1, a, wq0, wk0, wq1, wk1, m, x0, x1) do { u32 t_; asm volatile( \
    "v_smfmac_f32_16x16x64_bf16 %0, %6, %7, %11\n\tv_and_b32 %5, 0x7fff7fff, %12\n\t" \
    "v_smfmac_f32_16x16x64_bf16 %1, %6, %8, %11\n\tv_pk_max_u16 %4, %4, %5\n\t" \
    "v_smfmac_f32_16x16x64_bf16 %2, %6, %9, %11\n\tv_and_b32 %5, 0x7fff7fff, %13\n\t" \
    "v_smfmac_f32_16x16x64_bf16 %3, %6, %10, %11\n\tv_pk_max_u16 %4, %4, %5" \
    : "+v"(q0), "+v"(k0), "+v"(q1), "+v"(k1), "+v"(m), "=&v"(t_) : "v"(a), "a"(wq0), "a"(wk0), "a"(wq1), "a"(wk1), "v"(idx), "v"(x0), "v"(x1)); } while (0)
#define MF16Z(acc, a, b) asm volatile("v_mfma_f32_16x16x16_bf16 %0, %1, %2, 0" : "=&v"(acc) : "v"(a), "v"(b))
#define MF32N(acc, a, b) asm volatile("s_nop 1\n\tv_mfma_f32_16x16x32_bf16 %0, %1, %2, %0" : "+v"(acc) : "v"(a), "v"(b))
#define MF16(acc, a, b) asm volatile("v_mfma_f32_16x16x16_bf16 %0, %1, %2, %0" : "+v"(acc) : "v"(a), "v"(b))
#define MF32C(acc, a, b, c) asm volatile("v_mfma_f32_16x16x32_bf16 %0, %1, %2, %3" : "=&v"(acc) : "v"(a), "v"(b), "v"(c))
DI float max12(const f32x4& a, const f32x4& b, const f32x4& c) {
    float t0, t1, t2;
    asm("v_max3_f32 %0, %3, %4, %5\n\tv_max3_f32 %1, %6, %7, %8\n\tv_max3_f32 %2, %9, %10, %11\n\tv_max3_f32 %0, %0, %12, %13\n\tv_max3_f32 %1, %1, %2, %14\n\tv_max_f32 %0, %0, %1"
        : "=&v"(t0), "=&v"(t1), "=&v"(t2) : "v"(a[0]), "v"(a[1]), "v"(a[2]), "v"(a[3]), "v"(b[0]), "v"(b[1]), "v"(b[2]), "v"(b[3]), "v"(c[0]), "v"(c[1]), "v"(c[2]), "v"(c[3]));
    return t0;
}
DI int cvt_rpi(float v) { int r; asm("v_cvt_rpi_i32_f32 %0, %1" : "=v"(r) : "v"(v)); return r; }
#define ZERO4(acc) asm volatile("v_mov_b32 %0, 0\n\tv_mov_b32 %1, 0\n\tv_mov_b32 %2, 0\n\tv_mov_b32 %3, 0" : "=v"(acc[0]), "=v"(acc[1]), "=v"(acc[2]), "=v"(acc[3]))
#define MFMA_DONE2(a, b) asm volatile("s_nop 7" : "+v"(a), "+v"(b))
DI u32x2 trd(ldsp p) { return __builtin_bit_cast(u32x2, __builtin_amdgcn_ds_read_tr16_b64_v4i16((LAS s16x4*)p)); }
DI u32x4 cat(u32x2 a, u32x2 b) { return (u32x4){a.x, a.y, b.x, b.y}; }
DI u32x4 ldsr4(ldsp p) { return *(LAS u32x4*)p; }
DI bf16x16 ldsr8(ldsp p) { const u32x4 a = ldsr4(p), b = ldsr4(p + 1024); return __builtin_bit_cast(bf16x16, (u32x8){a.x, a.y, a.z, a.w, b.x, b.y, b.z, b.w}); }
DI u64 clk() { u64 t; asm volatile("s_memtime %0\n\ts_waitcnt lgkmcnt(0)" : "=s"(t) :: "memory"); return t; }
#define SB() __builtin_amdgcn_sched_barrier(0)
DI u32x4 gld4(const void* p) { return *(const GAS u32x4*)p; }
DI void gst4(void* p, u32x4 v) { *(GAS u32x4*)p = v; }
#define WAIT_V(n) asm volatile("s_waitcnt vmcnt(" #n ")" ::: "memory")
#define WAIT_L0() asm volatile("s_waitcnt lgkmcnt(0)" ::: "memory")
#define BAR() do { asm volatile("s_waitcnt lgkmcnt(0)" ::: "memory"); __builtin_amdgcn_s_barrier(); asm volatile("" ::: "memory"); } while (0)
DI float swap32_sum(float a, float b) {
    auto r = __builtin_amdgcn_permlane32_swap(__builtin_bit_cast(u32, a), __builtin_bit_cast(u32, b), false, false);
    const u32 r0 = r[0], r1 = r[1];
    return __builtin_bit_cast(float, r0) + __builtin_bit_cast(float, r1);
}
DI u32 opaque(u32 v) { asm("" : "+v"(v)); return v; }
DI int lane_id() { return (int)opaque(__builtin_amdgcn_mbcnt_hi(~0u, __builtin_amdgcn_mbcnt_lo(~0u, opaque(0u)))); }
DI f32x2 sw16(float v) { const u32 a = __builtin_bit_cast(u32, v); auto r = __builtin_amdgcn_permlane16_swap(a, opaque(a), false, false); const u32 r0 = r[0], r1 = r[1]; return (f32x2){__builtin_bit_cast(float, r0), __builtin_bit_cast(float, r1)}; }
DI f32x2 sw32(float v) { const u32 a = __builtin_bit_cast(u32, v); auto r = __builtin_amdgcn_permlane32_swap(a, opaque(a), false, false); const u32 r0 = r[0], r1 = r[1]; return (f32x2){__builtin_bit_cast(float, r0), __builtin_bit_cast(float, r1)}; }
DI float xmax16(float v) { const f32x2 r = sw16(v); return vmax(r.x, r.y); }
DI float xmax32(float v) { const f32x2 r = sw32(v); return vmax(r.x, r.y); }
DI float xsum16(float v) { const f32x2 r = sw16(v); return r.x + r.y; }
DI float xsum32(float v) { const f32x2 r = sw32(v); return r.x + r.y; }
template <int CTRL> DI u32 dpp(u32 v) { return (u32)__builtin_amdgcn_update_dpp(0, (int)v, CTRL, 0xf, 0xf, true); }
template <int CTRL> DI float dppf(float old, float v) { return __builtin_bit_cast(float, __builtin_amdgcn_update_dpp(__builtin_bit_cast(int, old), __builtin_bit_cast(int, v), CTRL, 0xf, 0xf, false)); }
template <int CTRL> DI float dppf(float v) { return __builtin_bit_cast(float, dpp<CTRL>(__builtin_bit_cast(u32, v))); }
DI float wave_sum(float v) {
    v += dppf<0xB1>(v);
    v += dppf<0x4E>(v);
    v += dppf<0x124>(v);
    v += dppf<0x128>(v);
    v = xsum16(v); v = xsum32(v);
    return v;
}
DI float wave_max(float v) {
    v = fmaxf(v, dppf<0xB1>(v)); v = fmaxf(v, dppf<0x4E>(v)); v = fmaxf(v, dppf<0x124>(v)); v = fmaxf(v, dppf<0x128>(v));
    v = xmax16(v); v = xmax32(v);
    return v;
}

DI u32 wave_umax(u32 v) {
    v = max(v, dpp<0xB1>(v)); v = max(v, dpp<0x4E>(v)); v = max(v, dpp<0x124>(v)); v = max(v, dpp<0x128>(v));
    { auto r = __builtin_amdgcn_permlane16_swap(v, opaque(v), false, false); const u32 a = r[0], b = r[1]; v = max(a, b); }
    { auto r = __builtin_amdgcn_permlane32_swap(v, opaque(v), false, false); const u32 a = r[0], b = r[1]; v = max(a, b); }
    return v;
}

typedef __amdgpu_buffer_rsrc_t rsrc_t;
DI rsrc_t mk_rsrc(const void* p, u32 bytes) { return __builtin_amdgcn_make_buffer_rsrc((void*)p, 0, (int)bytes, 0x00020000); }
DI void st16(u32x4 v, rsrc_t r, u32 voff, u32 soff) { __builtin_amdgcn_raw_buffer_store_b128(v, r, voff, soff, ST_AUX); asm volatile("s_nop 1" :: "v"(v) : "memory"); }
#define DMA16(r, voff, soff, ldsa, LO, GO) asm volatile("s_add_u32 m0, %0, %4\n\ts_nop 0\n\tbuffer_load_dwordx4 %1, %2, %3 offen offset:%5 lds" \
    :: "s"(ldsa), "v"(voff), "s"(r), "s"(soff), "n"((LO) - (GO)), "n"(GO) : "memory", "scc")
DI u32 chunk_voff(int lane, int wave, u32 ldb, bool perm, int rg) {
    const int r7 = (lane & 31) >> 2;
    const int rl = 32 * wave + (perm ? 16 * (r7 >> 2) + (r7 & 3) + 4 * rg : r7 + 8 * rg);
    return rl * ldb + (4 * (lane >> 5) + ((lane & 3) ^ ((2 * rg + (r7 >> 2)) & 3))) * 16;
}
struct CV { u32 v[4]; };
DI CV make_cv(int lane, int wave, u32 ldb, bool perm) { CV c; for (int i = 0; i < 4; ++i) c.v[i] = chunk_voff(lane, wave, ldb, perm, i); return c; }
#define CHUNK_PIECE(r, cv, soff, lw, LO, i) DMA16(r, cv.v[(i) >> 1], soff, lw, (LO) + (i) * 1024, ((i) & 1) * 128)
template <int LO, int I> DI void chunk_piece(rsrc_t r, const CV& cv, u32 soff, u32 lw) { CHUNK_PIECE(r, cv, soff, lw, LO, I); }
#define ALD4(dst, r, voff, soff, GO, AUX) asm volatile("buffer_load_dwordx4 %0, %1, %2, %3 offen offset:%4" AUX : "=v"(dst) : "v"(voff), "s"(r), "s"(soff), "n"(GO) : "memory")
#define PIN8(a, o) asm volatile("" : "+v"(a[o]), "+v"(a[o + 1]), "+v"(a[o + 2]), "+v"(a[o + 3]), "+v"(a[o + 4]), "+v"(a[o + 5]), "+v"(a[o + 6]), "+v"(a[o + 7]))
template <int N> DI void wait_v() { asm volatile("s_waitcnt vmcnt(%0)" :: "n"(N) : "memory"); }
#define JLOG(k)
#define JSET(v)
#define LOGT(slot, k)
DI GAS u32* ctlp(KP p, u32 off) { return (GAS u32*)(p->ws + OFF_CTL + off); }
DI u32 ld_dev(GAS u32* q) { return __hip_atomic_load(q, RLX_AGENT); }
template <class F> DI void wg_wait(KP p, int wave, u32 code, F&& left) {
    if (wave == 0) {
        for (u32 spins = 0;; ++spins) {
            const int d = left();
            if (__all(d <= 0)) break;
            if (spins > SPIN_CAP) { __hip_atomic_store(ctlp(p, CTL_TMO), code, RLX_AGENT); break; }
            if (__any(d > 1)) __builtin_amdgcn_s_sleep(40); else __builtin_amdgcn_s_sleep(3);
        }
    }
    BAR();
}
#define DRAIN_BAR() do { wait_v<0>(); BAR(); } while (0)
DI u32 tr_base(int lane, int c1, int a) {
    const int g = lane >> 4, q = (lane & 15) >> 2, p = lane & 3;
    return 2048u * g + 64u * (4 * a + q) + 16u * ((2 * c1 + (p >> 1)) ^ ((2 * g + a) & 3)) + 8u * (p & 1);
}
struct TrB { u32 b[2][2]; };
DI TrB make_trb(int lane) { TrB t; t.b[0][0] = tr_base(lane, 0, 0); t.b[0][1] = tr_base(lane, 0, 1); t.b[1][0] = tr_base(lane, 1, 0); t.b[1][1] = tr_base(lane, 1, 1); return t; }
DI u32x4 wfrag(ldsp buf, const TrB& t, int ks, int ct) {
    const int o = 8192 * ks + 512 * (ct >> 1);
    return cat(trd(buf + t.b[ct & 1][0] + o), trd(buf + t.b[ct & 1][1] + o));
}
DI u32x4 pack2(const f32x4& a, const f32x4& b) { return (u32x4){pk(a[0], a[1]), pk(a[2], a[3]), pk(b[0], b[1]), pk(b[2], b[3])}; }

__global__ void __launch_bounds__(256) k_mod(Params p) {
    __shared__ float red[32][65];
    const int tid = threadIdx.x, b = blockIdx.x;
    if (b >= 48) {
        const u32x4 z = {0u, 0u, 0u, 0u};
        if (b < 112) { for (int i = 0; i < 8; ++i) gst4(p.ws + OFF_ACC + (size_t)(b - 48) * 32768 + (i * 256 + tid) * 16, z); }
        else { for (int i = 0; i < 16; ++i) gst4(p.ws + OFF_CTL + (i * 256 + tid) * 16, z); }
        return;
    }
    const int cg = tid & 7, rg = tid >> 3;
    float acc[8] = {0, 0, 0, 0, 0, 0, 0, 0};
#pragma unroll 4
    for (int i = 0; i < 16; ++i) {
        const int r = rg + 32 * i;
        const float cv = lo_f(p.c[r]);
        const float s = rbf(cv / (1.f + __expf(-cv)));
        const u32x4 w = gld4(p.wmod + (size_t)r * 3072 + b * 64 + cg * 8);
#pragma unroll
        for (int j = 0; j < 4; ++j) { acc[2 * j] += s * lo_f(w[j]); acc[2 * j + 1] += s * hi_f(w[j]); }
    }
#pragma unroll
    for (int j = 0; j < 8; ++j) red[rg][cg * 8 + j] = acc[j];
    __syncthreads();
    if (tid < 64) {
        float s = 0;
        for (int i = 0; i < 32; ++i) s += red[i][tid];
        ((float*)(p.ws + OFF_MOD))[b * 64 + tid] = rbf(s);
    }
}

struct LnP { float g[8], s[8]; };
DI LnP ln_params(const float* shift, const float* scale, int lane) {
    LnP q;
    const f32x4 a = *(const GAS f32x4*)(scale + lane * 8), b = *(const GAS f32x4*)(scale + lane * 8 + 4), c = *(const GAS f32x4*)(shift + lane * 8), d = *(const GAS f32x4*)(shift + lane * 8 + 4);
#pragma unroll
    for (int j = 0; j < 4; ++j) { q.g[j] = rbf(1.f + a[j]); q.g[4 + j] = rbf(1.f + b[j]); q.s[j] = c[j]; q.s[4 + j] = d[j]; }
    return q;
}
template <int N> DI void ln_rows(u32x4 (&v)[N], const LnP& q) {
    float x[N][8], m[N], r[N];
#pragma unroll
    for (int i = 0; i < N; ++i) {
#pragma unroll
        for (int j = 0; j < 4; ++j) { x[i][2 * j] = lo_f(v[i][j]); x[i][2 * j + 1] = hi_f(v[i][j]); }
        m[i] = ((x[i][0] + x[i][1]) + (x[i][2] + x[i][3])) + ((x[i][4] + x[i][5]) + (x[i][6] + x[i][7]));
    }
#pragma unroll
    for (int i = 0; i < N; ++i) m[i] = wave_sum(m[i]) * (1.f / DM);
#pragma unroll
    for (int i = 0; i < N; ++i) {
        r[i] = 0;
#pragma unroll
        for (int j = 0; j < 8; ++j) r[i] += (x[i][j] - m[i]) * (x[i][j] - m[i]);
    }
#pragma unroll
    for (int i = 0; i < N; ++i) r[i] = __builtin_amdgcn_rcpf(rbf(__builtin_amdgcn_sqrtf(rbf(rbf(wave_sum(r[i]) * (1.f / DM)) + 1e-5f))));
#pragma unroll
    for (int i = 0; i < N; ++i) {
        const float mb = rbf(m[i]);
#pragma unroll
        for (int j = 0; j < 4; ++j) {
            u32 t = pk(x[i][2 * j] - mb, x[i][2 * j + 1] - mb);
            t = pk(lo_f(t) * r[i], hi_f(t) * r[i]);
            t = pk(lo_f(t) * q.g[2 * j], hi_f(t) * q.g[2 * j + 1]);
            v[i][j] = pk(lo_f(t) + q.s[2 * j], hi_f(t) + q.s[2 * j + 1]);
        }
    }
}
DI u32 frag_off(int row, int lane) { return (((row >> 4) * 16 + (lane >> 2)) * 64 + (lane & 3) * 16 + (row & 15)) * 16; }
__global__ void __launch_bounds__(256) k_ln(Params p) {
    const int lane = threadIdx.x & 63, wave = threadIdx.x >> 6;
    const float* mod = (const float*)(p.ws + OFF_MOD);
    if (blockIdx.x == 0 && wave < 2) {
        float v[8];
#pragma unroll
        for (int j = 0; j < 8; ++j) { const int d = 32 * (lane >> 2) + 16 * (j >> 2) + 4 * (lane & 3) + (j & 3); v[j] = wave ? mod[3 * DM + d] : 1.f + mod[4 * DM + d]; }
        gst4(p.ws + OFF_LN2 + wave * 1024 + lane * 16, (u32x4){pk(v[0], v[1]), pk(v[2], v[3]), pk(v[4], v[5]), pk(v[6], v[7])});
    }
    const int row = blockIdx.x * 8 + wave * 2;
    u32x4 v[2] = {gld4(p.x + (size_t)row * DM + lane * 8), gld4(p.x + (size_t)(row + 1) * DM + lane * 8)};
    ln_rows<2>(v, ln_params(mod, mod + DM, lane));
    gst4(p.ws + OFF_H1F + frag_off(row, lane), v[0]); gst4(p.ws + OFF_H1F + frag_off(row + 1, lane), v[1]);
}

template <class G1, class G2>
DI void load_w(u32x4 (&w)[64], rsrc_t r1, const CV& c1, u32 o1, u32 s1, rsrc_t r2, const CV& c2, u32 o2, u32 s2, ldsp lds, u32 lw, G1&& g1, G2&& g2) {
    sfor<4>([&](auto cc) { sfor<8>([&](auto pc) { chunk_piece<cc.value * 32768, pc.value>(r1, c1, o1 + cc.value * s1, lw); }); });
    sfor<8>([&](auto ic) {
        constexpr int m = ic.value >> 2, cc = ic.value & 3;
        wait_v<(m == 0 ? 24 : 24 - 8 * cc)>(); BAR();
#pragma unroll
        for (int i = 0; i < 8; ++i) w[32 * m + 8 * cc + i] = m == 0 ? g1(lds + cc * 32768, i) : g2(lds + cc * 32768, i);
        BAR();
        if constexpr (m == 0) sfor<8>([&](auto pc) { chunk_piece<cc * 32768, pc.value>(r2, c2, o2 + cc * s2, lw); });
    });
}
DI u32 ring_off(int s) { return (s & 3) == 0 ? 131072u : (u32)((s & 3) - 1) * 32768u; }
template <int TYPE>
DI void role_proj(KP p, int h, ldsp lds, int wave) {
    FRESH(p); FRESH(wave);
    const int lane = lane_id();
    const TrB trb = make_trb(lane);
    const rsrc_t rws = mk_rsrc(p->ws, (u32)WS_END);
    const u32 l0 = (u32)(size_t)lds, lw = l0 + wave * 8192, l16 = lane * 16;
    auto fetch = [&](int s) {
        const u32 so = (u32)OFF_H1F + s * 32768 + wave * 8192, la = lw + ring_off(s);
#pragma unroll
        for (int i = 0; i < 4; ++i) DMA16(rws, l16, so, la, i * 1024, i * 1024);
#pragma unroll
        for (int i = 0; i < 4; ++i) DMA16(rws, l16, so + 4096, la, 4096 + i * 1024, i * 1024);
    };
    fetch(0);
    u32x4 w[64];
    {
        const CV ca = make_cv(lane, wave, 8192, false);
        auto ga = [&](ldsp b, int i) { return wfrag(b + 512 * wave, trb, i >> 1, i & 1); };
        if constexpr (TYPE == 0) load_w(w, mk_rsrc(p->wq, 4u << 20), ca, h * 256, 1048576, mk_rsrc(p->wk, 4u << 20), ca, h * 256, 1048576, lds, lw, ga, ga);
        else {
            const CV co = make_cv(lane, wave, 1024, true);
            load_w(w, mk_rsrc(p->wv, 4u << 20), ca, h * 256, 1048576, mk_rsrc(p->wo, 4u << 20), co, h * 131072, 256, lds, lw, ga, ga);
        }
    }
    fetch(1); fetch(2);
    LOGT(2560 + blockIdx.x, 0);
    GAS u32* const flag = ctlp(p, CTL_FQK + h * 128 + TYPE * 4);
    constexpr u32 L_V = 98304;
    for (int s = 0; s < NSEG + TYPE; ++s) {
        if (TYPE == 0) { if (s < 2) wait_v<16>(); else if (s < NSEG - 2) wait_v<12>(); else wait_v<4>(); }
        else { if (s == 0) wait_v<16>(); else if (s < NSEG - 2) wait_v<8>(); else wait_v<0>(); }
        BAR();
        if (wave == 0 && lane == 0 && s > 1) __hip_atomic_store(flag, (u32)(s - 1), RLX_AGENT);
        if (s == 2) LOGT(2560 + blockIdx.x, 1);
        if (s == 10) LOGT(2560 + blockIdx.x, 2);
        if (TYPE == 1 && s > 0) {
            ldsp vb = lds + L_V + ((s - 1) & 1) * 8192 + l16;
            u32x4 vf[8];
#pragma unroll
            for (int i = 0; i < 8; ++i) vf[i] = ldsr4(vb + i * 1024);
#pragma unroll
            for (int t = 0; t < 8; ++t) {
                f32x4 a0 = {0, 0, 0, 0}, a1 = {0, 0, 0, 0};
#pragma unroll
                for (int ks = 0; ks < 4; ++ks) { const bf16x8 wf = as_bf(w[32 + 8 * (t >> 1) + 2 * ks + (t & 1)]); a0 = mfma32(as_bf(vf[ks]), wf, a0); a1 = mfma32(as_bf(vf[4 + ks]), wf, a1); }
                st16(pack2(a0, a1), rws, l16 + (t & 1) * 1024, (u32)OFF_UF + (((s - 1) * NH + h) * 32 + 8 * (t >> 1) + 2 * wave) * 1024);
            }
        }
        SB();
        if (s + 3 < NSEG) fetch(s + 3);
        SB();
        if (s < NSEG) {
            ldsp hb = lds + ring_off(s) + l16;
            constexpr int NT = TYPE == 0 ? 4 : 2;
            f32x4 acc[2][NT];
#pragma unroll
            for (int i = 0; i < 2 * NT; ++i) acc[i / NT][i % NT] = (f32x4){0, 0, 0, 0};
            u32x4 hf[4][2];
#pragma unroll
            for (int ks = 0; ks < 3; ++ks) { hf[ks][0] = ldsr4(hb + ks * 1024); hf[ks][1] = ldsr4(hb + 16384 + ks * 1024); }
#pragma unroll
            for (int ks = 0; ks < 16; ++ks) {
                SB();
                if (ks + 3 < 16) { hf[(ks + 3) & 3][0] = ldsr4(hb + (ks + 3) * 1024); hf[(ks + 3) & 3][1] = ldsr4(hb + 16384 + (ks + 3) * 1024); }
                SB();
#pragma unroll
                for (int t = 0; t < NT; ++t)
#pragma unroll
                    for (int tt = 0; tt < 2; ++tt) acc[tt][t] = mfma32(as_bf(w[32 * (t >> 1) + 2 * ks + (t & 1)]), as_bf(hf[ks & 3][tt]), acc[tt][t]);
            }
            if constexpr (TYPE == 0) {
#pragma unroll
                for (int i = 0; i < 4; ++i) st16(pack2(acc[i & 1][(i >> 1) * 2], acc[i & 1][(i >> 1) * 2 + 1]), rws, l16,
                                                                                   (u32)(i >> 1 ? OFF_KF : OFF_QF) + (s * NH + h) * 8192 + ((i & 1) * 4 + wave) * 1024);
            } else {
#pragma unroll
                for (int tt = 0; tt < 2; ++tt) *(LAS u32x4*)(lds + L_V + (s & 1) * 8192 + (tt * 4 + wave) * 1024 + l16) = pack2(acc[tt][0], acc[tt][1]);
            }
        }
    }
    DRAIN_BAR();
    if (wave == 0 && lane == 0) __hip_atomic_store(flag, (u32)NSEG, RLX_AGENT);
}

DI int dperm(int pos) { const int B = pos >> 4, tb = (pos >> 3) & 1, g = (pos >> 1) & 3, b = pos & 1; return 32 * (B >> 1) + 16 * tb + 4 * g + 2 * (B & 1) + b; }
DI int posl(int k) { return (k & 48) + 8 * ((k >> 1) & 1) + 2 * ((k >> 2) & 3) + (k & 1); }
constexpr int L_BS = 0, L_IMG = 131072, ROWB = 1088, L_FI = L_IMG + 8 * ROWB, L_SS = L_FI + 4096, L_END = 163840;

DI void chain_role(KP p, int h, int c, ldsp lds, const int wave) {
    const int lane = lane_id(), tid = wave * 64 + lane;
    const int g = lane >> 4, li = lane & 15;
    const TrB trb = make_trb(lane);
    const __amdgpu_buffer_rsrc_t rws = __builtin_amdgcn_make_buffer_rsrc(p->ws, 0, (int)WS_END, 0x00020000);
    bf16x16 wqk[2][2][8];
    u32 m0v[8];
    float mdv[16];
    {
        const rsrc_t rq = mk_rsrc(p->wq, 4u << 20), rk = mk_rsrc(p->wk, 4u << 20), rv = mk_rsrc(p->wv, 4u << 20), ro = mk_rsrc(p->wo, 4u << 20);
        const u32 lw = (u32)(size_t)lds + wave * 8192;
        CV cq;
#pragma unroll
        for (int rg = 0; rg < 4; ++rg) {
            const int r7 = (lane & 31) >> 2, rho = 32 * wave + 8 * rg + r7;
            cq.v[rg] = dperm((rho & 64) + posl(rho & 63)) * 8192 + (4 * (lane >> 5) + ((lane & 3) ^ ((2 * rg + (r7 >> 2)) & 3))) * 16;
        }
        const CV co = make_cv(lane, wave, 1024, false);
#pragma unroll
        for (int i = 0; i < 32; ++i) CHUNK_PIECE(rq, cq, (i >> 3) * 1048576 + h * 256, lw, (i >> 3) * 32768, i & 7);
#pragma unroll
        for (int i = 0; i < 8; ++i) CHUNK_PIECE(ro, co, h * 131072 + c * 256, lw, L_IMG, i);
        u32x4 av[32];
#pragma unroll
        for (int t = 0; t < 8; ++t) {
            const int tau = t & 3;
            const u32 vo = dperm(64 * (t >> 2) + posl(32 * (tau >> 1) + 8 * (li >> 2) + 4 * (tau & 1) + (li & 3))) * 8192 + g * 16;
#pragma unroll
            for (int ks = 0; ks < 4; ++ks) ALD4(av[4 * t + ks], rv, vo, wave * 1048576 + h * 256, ks * 64, "");
        }
        sfor<8>([&](auto ic) {
            constexpr int m = ic.value >> 2, cc = ic.value & 3;
            wait_v<(m == 0 ? 63 : 24 - 8 * cc)>(); BAR();
#pragma unroll
            for (int et = 0; et < 2; ++et)
#pragma unroll
                for (int k2 = 0; k2 < 2; ++k2) {
                    ldsp buf = lds + cc * 32768 + 512 * wave + 16384 * k2;
                    const u32x2 t0 = trd(buf + trb.b[et][0]), t1 = trd(buf + trb.b[et][1]), t2 = trd(buf + trb.b[et][0] + 8192), t3 = trd(buf + trb.b[et][1] + 8192);
                    wqk[m][et][2 * cc + k2] = __builtin_bit_cast(bf16x16, (u32x8){t0.x, t0.y, t1.x, t1.y, t2.x, t2.y, t3.x, t3.y});
                }
            if constexpr (m == 0) {
                BAR();
                sfor<8>([&](auto pc) { chunk_piece<cc * 32768, pc.value>(rk, cq, cc * 1048576 + h * 256, lw); });
            }
        });
        PIN8(av, 0); PIN8(av, 8); PIN8(av, 16); PIN8(av, 24);
        LOGT(2560 + blockIdx.x, 0);
#pragma unroll
        for (int i = 0; i < 8; ++i) m0v[i] = *(const GAS u32*)(p->mem0 + (tid >> 5) * DM + dperm((tid & 31) * 16 + 2 * i));
#pragma unroll
        for (int j = 0; j < 8; ++j) { mdv[j] = ((const float*)(p->ws + OFF_MOD))[lane * 8 + j]; mdv[8 + j] = ((const float*)(p->ws + OFF_MOD))[DM + lane * 8 + j]; }
        BAR();
        float ssv = 0;
        float ss2 = 0;
#pragma unroll 1
        for (int ct = 0; ct < 8; ++ct) {
            u32x4 wo[4];
            ldsp wb = lds + L_IMG + 512 * (ct >> 1);
            const u32 b0 = (ct & 1) ? trb.b[1][0] : trb.b[0][0], b1 = (ct & 1) ? trb.b[1][1] : trb.b[0][1];
#pragma unroll
            for (int ks = 0; ks < 4; ++ks) wo[ks] = cat(trd(wb + b0 + 8192 * ks), trd(wb + b1 + 8192 * ks));
            float s = 0;
#pragma unroll
            for (int u = 0; u < 4; ++u) {
                f32x4 a0, a1;
                MF32ZN(a0, av[8 * u], wo[0]); MF32Z(a1, av[8 * u + 4], wo[0]);
#pragma unroll
                for (int ks = 1; ks < 4; ++ks) { MF32(a0, av[8 * u + ks], wo[ks]); MF32(a1, av[8 * u + 4 + ks], wo[ks]); }
                MFMA_DONE2(a0, a1);
                s += a0[0] * a0[0] + a0[1] * a0[1] + a0[2] * a0[2] + a0[3] * a0[3] + a1[0] * a1[0] + a1[1] * a1[1] + a1[2] * a1[2] + a1[3] * a1[3];
                *(LAS u32x4*)(lds + L_BS + ((ct * 8 + 2 * wave + (u >> 1)) * 2 + (u & 1)) * 1024 + lane * 16) = pack2(a0, a1);
            }
            s = xsum32(xsum16(s));
            if ((ct & 3) == g) { if (ct < 4) ssv = s; else ss2 = s; }
        }
        BAR();
        *(LAS float*)(lds + L_SS + (wave * 128 + lane) * 4) = ssv;
        *(LAS float*)(lds + L_SS + (wave * 128 + 64 + lane) * 4) = ss2;
        *(LAS u32x4*)(lds + L_IMG + (tid >> 5) * ROWB + (tid & 31) * 32) = (u32x4){m0v[0], m0v[1], m0v[2], m0v[3]};
        *(LAS u32x4*)(lds + L_IMG + (tid >> 5) * ROWB + (tid & 31) * 32 + 16) = (u32x4){m0v[4], m0v[5], m0v[6], m0v[7]};
        BAR();
        if (wave == 0) {
            const LAS float* sp = (const LAS float*)(lds + L_SS);
            const float a = (sp[lane] + sp[128 + lane]) + (sp[256 + lane] + sp[384 + lane]);
            const float b2 = (sp[64 + lane] + sp[192 + lane]) + (sp[320 + lane] + sp[448 + lane]);
            const float mx = wave_max(fmaxf(a, b2));
            if (lane == 0) __hip_atomic_store((GAS u32*)(p->ws + OFF_CTL + CTL_BMX) + (h * 4 + c), __builtin_bit_cast(u32, mx) | 0x80000000u, RLX_AGENT);
        }
    }
    LOGT(2560 + blockIdx.x, 1);
    const int idx = li < 8 ? 0x4444 : 0xEEEE;
    const u32 a_off = opaque(L_IMG + (li & 7) * ROWB + 32 * g + 16 * (li >> 3));
    const u32 fw_off = opaque(L_FI + (32 * wave + li) * 32 + 8 * (g ^ ((li >> 2) & 3)));
    const u32 fr_off = opaque(L_FI + (4 * g + (li >> 2)) * 32 + 8 * ((li & 3) ^ (g & 3)));
    const u32 bs_off = opaque(L_BS + (2 * wave) * 16384 + lane * 16);
    const int slot = (li & 7) * 8 + (li >> 3) * 4 + g;
    const u32 acc_off = (((c * 4 + wave) * 2) * 4 + (slot >> 4)) * 256 + (slot & 15) * 8;
    const u32 gat_off = opaque((lane >> 3) * 256 + (lane & 7) * 16), gat_s = (u32)OFF_ACC + wave * 8192;
    const u32 img_w = opaque(L_IMG + (2 * ((lane >> 3) & 3) + ((lane >> 2) & 1)) * ROWB + 32 * (wave * 8 + (lane >> 5)) + 8 * (lane & 3));
    const u32 l16 = opaque(lane * 16);
    const u32 kx_off = (u32)OFF_KF + h * 8192, ux_off = (u32)OFF_UF + (h * 32 + c * 8 + wave * 2) * 1024, qx_off = (u32)OFF_QF + h * 8192 + (c & 1) * 4096;
    const u32 z_lane = opaque((g >> 1) * 1024 + ((h & 3) * 16 + li) * 16 + (g & 1) * 8), z_s = (u32)OFF_ZF + ((h >> 2) * 32 + c * 8 + wave * 2) * 1024;
    const u32 xm_off = opaque(L_IMG + (2 * wave + (lane >> 5)) * ROWB + (lane & 31) * 32);
    const u32 mxw_off = opaque(L_SS + tid * 4), mxr_off = opaque(L_SS + lane * 16);
    const bool pjob = (c < 2) && (wave == 1);
    f32x4 cneg, cx0, cx1;
    u32x4 ones = {0x3f803f80u, 0x3f803f80u, 0x3f803f80u, 0x3f803f80u};
#pragma unroll
    for (int j = 0; j < 4; ++j) {
        cneg[j] = g >= 2 ? 0.f : -3.0e38f;
        cx0[j] = 4 * g + j <= c * 16 + li ? 0.f : -3.0e38f;
        cx1[j] = 16 + 4 * g + j <= c * 16 + li ? 0.f : -3.0e38f;
    }
    asm volatile("" : "+v"(cneg), "+v"(ones));
    if (pjob) { *(LAS f32x4*)(lds + L_SS + 2048 + l16) = cx0; *(LAS f32x4*)(lds + L_SS + 3072 + l16) = cx1; }
    bf16x16 b0r[8];
#pragma unroll
    for (int kb = 0; kb < 8; ++kb) b0r[kb] = ldsr8(lds + bs_off + kb * 2048);
    constexpr int L_KX = 0, L_QX = 32768;
    const u32 t16 = opaque(tid * 16);
    u32x4 pk0, pk1, pq, ux[2];
    GAS u64* const fqk = (GAS u64*)ctlp(p, CTL_FQK + h * 128);
    auto have = [](u64 f) { return (int)__builtin_amdgcn_readfirstlane(min((u32)f, (u32)(f >> 32))); };
    int fseen = 0;
    const bool lead = h == 0 && c == 3 && wave == 3;
    auto wait_proj = [&](int sg) {
        for (u32 spins = 0; (fseen = have(__hip_atomic_load(fqk, RLX_AGENT))) <= sg; ++spins) {
            if (spins > SPIN_CAP) { __hip_atomic_store(ctlp(p, CTL_TMO), 3u, RLX_AGENT); break; }
            __builtin_amdgcn_s_sleep(2);
        }
    };
    wait_proj(0);
    LOGT(2560 + blockIdx.x, 2);
    pk0 = __builtin_amdgcn_raw_buffer_load_b128(rws, t16, kx_off, 16);
    pk1 = __builtin_amdgcn_raw_buffer_load_b128(rws, t16, kx_off + 4096, 16);
    pq = __builtin_amdgcn_raw_buffer_load_b128(rws, t16, qx_off, 16);
#pragma unroll
    for (int t = 0; t < 2; ++t) ux[t] = __builtin_amdgcn_raw_buffer_load_b128(rws, l16 + t * 1024, ux_off, 16);
    float hmax, bmax;
    {
        float gm = 0, sq = 0;
#pragma unroll
        for (int j = 0; j < 8; ++j) { gm = fmaxf(gm, fabsf(1.f + mdv[8 + j])); sq += mdv[j] * mdv[j]; }
        hmax = 22.7f * wave_max(gm) + sqrtf(wave_sum(sq));
        u32 v0, v1;
        for (u32 spins = 0;; ++spins) {
            v0 = ld_dev(ctlp(p, CTL_BMX) + lane); v1 = ld_dev(ctlp(p, CTL_BMX) + 64 + lane);
            if (__all((v0 & v1) >> 31)) break;
            if (spins > SPIN_CAP) { __hip_atomic_store(ctlp(p, CTL_TMO), 1u, RLX_AGENT); break; }
            __builtin_amdgcn_s_sleep(2);
        }
        bmax = 1.1f * sqrtf(wave_max(fmaxf(__builtin_bit_cast(float, v0 & 0x7fffffffu), __builtin_bit_cast(float, v1 & 0x7fffffffu))));
    }
    LOGT(2560 + blockIdx.x, 3);
    const u32 thr = __builtin_bit_cast(u32, hmax * (1.f / 22.7f)) >> 16;
    const u32 eb0 = __builtin_amdgcn_readfirstlane(min(max(__builtin_bit_cast(u32, bmax * hmax) >> 23, 26u), 254u));
    BAR();
    *(LAS u32x4*)(lds + L_KX + t16) = pk0; *(LAS u32x4*)(lds + L_KX + 4096 + t16) = pk1; *(LAS u32x4*)(lds + L_QX + t16) = pq;
#define STAMP(i) SB()
#define STAMPC(i) STAMP(i)
    auto step = [&](const int s, auto last_tag) {
        constexpr bool LAST = decltype(last_tag)::value;
        f32x4 aq[2][2], az[2];
        u32 mx2 = 0;
        aq[0][0] = aq[0][1] = aq[1][0] = aq[1][1] = az[0] = az[1] = (f32x4){0, 0, 0, 0};
        asm volatile("" : "+v"(aq[0][0]), "+v"(aq[0][1]), "+v"(aq[1][0]), "+v"(aq[1][1]), "+v"(az[0]), "+v"(az[1]), "+v"(mx2));
        BAR();
        STAMP(0);
        if (lead && lane < 8) __hip_atomic_store(ctlp(p, CTL_PROG + lane * 128), (u32)s, RLX_AGENT);
        const bool poll = !LAST && fseen <= s + 1;
        u64 fnext = 0;
        if (poll) fnext = __hip_atomic_load(fqk, RLX_AGENT);
        u32x4 af[8], xm[2];
        bf16x16 b1[8];
#pragma unroll
        for (int kb = 0; kb < 8; ++kb) af[kb] = ldsr4(lds + a_off + 128 * kb);
        xm[0] = ldsr4(lds + xm_off); xm[1] = ldsr4(lds + xm_off + 16);
        SB();
        u32x4 bh[16];
#pragma unroll
        for (int kb = 0; kb < 4; ++kb)
#pragma unroll
            for (int j = 0; j < 4; ++j) {
                SMF_A(aq[j & 1][j >> 1], af[kb], wqk[j & 1][j >> 1][kb]);
                bh[4 * kb + j] = ldsr4(lds + bs_off + 16384 + (4 * kb + j) * 1024);
                SB();
            }
#pragma unroll
        for (int kb = 0; kb < 8; ++kb) b1[kb] = __builtin_bit_cast(bf16x16, (u32x8){bh[2 * kb].x, bh[2 * kb].y, bh[2 * kb].z, bh[2 * kb].w, bh[2 * kb + 1].x, bh[2 * kb + 1].y, bh[2 * kb + 1].z, bh[2 * kb + 1].w});
#pragma unroll
        for (int kb = 4; kb < 8; ++kb) SMF4_M(aq[0][0], aq[1][0], aq[0][1], aq[1][1], af[kb], wqk[0][0][kb], wqk[1][0][kb], wqk[0][1][kb], wqk[1][1][kb], mx2, xm[(kb >> 1) & 1][2 * (kb & 1)], xm[(kb >> 1) & 1][2 * (kb & 1) + 1]);
        STAMP(1);
        SMF_V(az[1], af[0], b1[0]);
        asm volatile("s_nop 2" : "+v"(aq[0][0]), "+v"(aq[1][0]));
#pragma unroll
        for (int et = 0; et < 2; ++et) {
            float r[4];
            if (et == 1) asm volatile("" : "+v"(aq[0][1]), "+v"(aq[1][1]));
#pragma unroll
            for (int j = 0; j < 4; ++j) r[j] = swap32_sum(aq[0][et][j], aq[1][et][j]);
            *(LAS u32x2*)(lds + fw_off + et * 512) = (u32x2){pk(r[0], r[1]), pk(r[2], r[3])};
            SB();
        }
        *(LAS u32*)(lds + mxw_off) = mx2;
        SB();
#pragma unroll
        for (int kb = 1; kb < 5; ++kb) SMF_V(az[1], af[kb], b1[kb]);
        SB();
        BAR();
        STAMP(2);
        u32x4 F[4], kx[2][4];
        const u32 kb_off = l16 + L_KX + (s & 1) * 8192;
#pragma unroll
        for (int ks = 0; ks < 4; ++ks) {
            F[ks] = cat(trd(lds + fr_off + ks * 1024), trd(lds + fr_off + ks * 1024 + 512));
            kx[0][ks] = ldsr4(lds + kb_off + ks * 1024); kx[1][ks] = ldsr4(lds + kb_off + 4096 + ks * 1024);
            if (ks == 1) { SB(); SMF_V(az[1], af[5], b1[5]); }
            if (ks == 2) SMF_V(az[1], af[6], b1[6]);
            if (ks == 3) SMF_V(az[1], af[7], b1[7]);
            SB();
        }
        const u32x4 mx4 = ldsr4(lds + mxr_off);
        SB();
        u32x2 zd[2];
        u32 zt[4], zu[4];
        SMF_V(az[0], af[0], b0r[0]); SMF_V(az[0], af[1], b0r[1]);
        asm volatile("s_nop 5" : "+v"(az[1]));
#pragma unroll
        for (int j = 0; j < 4; ++j) { const float t = az[1][j]; zt[j] = __builtin_bit_cast(u32, t); zu[j] = opaque(zt[j]); }
        SB();
#pragma unroll
        for (int j = 0; j < 4; ++j) {
            SMF_V(az[0], af[2 + j], b0r[2 + j]);
            auto r = __builtin_amdgcn_permlane32_swap(zt[j], zu[j], false, false); zt[j] = r[0]; zu[j] = r[1];
            SB();
        }
        SMF_V(az[0], af[6], b0r[6]);
        float z1a = __builtin_bit_cast(float, zt[0]) + __builtin_bit_cast(float, zu[0]), z1b = __builtin_bit_cast(float, zt[1]) + __builtin_bit_cast(float, zu[1]);
        asm volatile("" : "+v"(z1a), "+v"(z1b));
        SB();
        SMF_V(az[0], af[7], b0r[7]);
        float z1c = __builtin_bit_cast(float, zt[2]) + __builtin_bit_cast(float, zu[2]), z1d = __builtin_bit_cast(float, zt[3]) + __builtin_bit_cast(float, zu[3]);
        asm volatile("" : "+v"(z1c), "+v"(z1d));
        SB();
        u32x2 zc;
        float sc_out = 0.f;
        u64 t_pub = 0;
        if (!LAST) {
            f32x4 st[3];
            MF32Z(st[1], kx[0][0], F[0]);
            zd[1] = (u32x2){pk(z1a, z1b), pk(z1c, z1d)};
            asm volatile("" : "+v"(zd[1]));
            SB();
            MF32Z(st[2], kx[1][0], F[0]);
            const u16x2 m2a = __builtin_elementwise_max(__builtin_bit_cast(u16x2, mx4.x + 0u), __builtin_bit_cast(u16x2, mx4.y + 0u)),
                        m2b = __builtin_elementwise_max(__builtin_bit_cast(u16x2, mx4.z + 0u), __builtin_bit_cast(u16x2, mx4.w + 0u));
            SB();
            MF32C(st[0], F[0], F[0], cneg);
            const u16x2 m2 = __builtin_elementwise_max(m2a, m2b);
            const u32 m1 = max((u32)m2.x, (u32)m2.y);
            SB();
            MF32(st[1], kx[0][1], F[1]);
            const bool big = __any(m1 >= thr);
            SB();
            MF32(st[2], kx[1][1], F[1]);
            asm volatile("" : "+v"(az[0]));
            { const float t0 = az[0][0], t1 = az[0][1]; zt[0] = __builtin_bit_cast(u32, t0); zu[0] = opaque(zt[0]); zt[1] = __builtin_bit_cast(u32, t1); zu[1] = opaque(zt[1]); }
            SB();
            MF32(st[0], F[1], F[1]);
            { const float t0 = az[0][2], t1 = az[0][3]; zt[2] = __builtin_bit_cast(u32, t0); zu[2] = opaque(zt[2]); zt[3] = __builtin_bit_cast(u32, t1); zu[3] = opaque(zt[3]); }
            SB();
            MF32(st[1], kx[0][2], F[2]);
            { auto r = __builtin_amdgcn_permlane32_swap(zt[0], zu[0], false, false); zt[0] = r[0]; zu[0] = r[1]; }
            { auto r = __builtin_amdgcn_permlane32_swap(zt[1], zu[1], false, false); zt[1] = r[0]; zu[1] = r[1]; }
            SB();
            MF32(st[2], kx[1][2], F[2]);
            { auto r = __builtin_amdgcn_permlane32_swap(zt[2], zu[2], false, false); zt[2] = r[0]; zu[2] = r[1]; }
            { auto r = __builtin_amdgcn_permlane32_swap(zt[3], zu[3], false, false); zt[3] = r[0]; zu[3] = r[1]; }
            SB();
            MF32(st[0], F[2], F[2]);
            z1a = __builtin_bit_cast(float, zt[0]) + __builtin_bit_cast(float, zu[0]); z1b = __builtin_bit_cast(float, zt[1]) + __builtin_bit_cast(float, zu[1]);
            asm volatile("" : "+v"(z1a), "+v"(z1b));
            SB();
            MF32(st[0], F[3], F[3]);
            z1c = __builtin_bit_cast(float, zt[2]) + __builtin_bit_cast(float, zu[2]); z1d = __builtin_bit_cast(float, zt[3]) + __builtin_bit_cast(float, zu[3]);
            asm volatile("" : "+v"(z1c), "+v"(z1d));
            SB();
            MF32(st[1], kx[0][3], F[3]);
            zd[0] = (u32x2){pk(z1a, z1b), pk(z1c, z1d)};
            asm volatile("" : "+v"(zd[0]));
            SB();
            MF32(st[2], kx[1][3], F[3]);
            u32 eb = eb0;
            if (big) eb = __builtin_amdgcn_readfirstlane(min(max(__builtin_bit_cast(u32, bmax * 22.7f * __builtin_bit_cast(float, (wave_umax(m1) + 1u) << 16)) >> 23, eb0), 254u));
            const float fsc = __builtin_bit_cast(float, (276u - eb) << 23);
            sc_out = __builtin_bit_cast(float, (eb - 25u) << 23);
            asm volatile("s_nop 5" : "+v"(st[0]), "+v"(st[1]), "+v"(st[2]));
            STAMP(3);
            float mxv = max12(st[0], st[1], st[2]);
            mxv = xmax32(xmax16(mxv));
            const float cs = SM_SCALE * LOG2E, nb = -mxv * cs;
#pragma unroll
            for (int j = 0; j < 4; ++j) st[0][j] = __builtin_amdgcn_exp2f(__builtin_fmaf(st[0][j], cs, nb));
            const u32x2 pm = {pk(st[0][0], st[0][1]), pk(st[0][2], st[0][3])};
            const u32x2 ones2 = {ones.x, ones.y};
            f32x4 o0, o1, os;
#pragma unroll
            for (int j = 0; j < 4; ++j) { st[1][j] = __builtin_fmaf(st[1][j], cs, nb); st[2][j] = __builtin_fmaf(st[2][j], cs, nb); }
            SB();
            MF16Z(os, ones2, pm);
            st[1][0] = __builtin_amdgcn_exp2f(st[1][0]); st[1][1] = __builtin_amdgcn_exp2f(st[1][1]);
            SB();
            MF16Z(o0, zd[0], pm);
            st[1][2] = __builtin_amdgcn_exp2f(st[1][2]); st[1][3] = __builtin_amdgcn_exp2f(st[1][3]);
            SB();
            MF16Z(o1, zd[1], pm);
#pragma unroll
            for (int j = 0; j < 4; ++j) st[2][j] = __builtin_amdgcn_exp2f(st[2][j]);
            const u32x4 px = pack2(st[1], st[2]);
            MF32N(os, ones, px); MF32(o0, ux[0], px); MF32(o1, ux[1], px);
            asm volatile("s_nop 5" : "+v"(os), "+v"(o0), "+v"(o1));
            const float f = fsc * __builtin_amdgcn_rcpf(os[0]);
            const float f8 = dppf<0x118>(f, f);
            int q[4];
#pragma unroll
            for (int j = 0; j < 4; ++j) {
                const float e0 = o0[j], e1 = o1[j];
                const float v = __builtin_bit_cast(float, __builtin_amdgcn_update_dpp(__builtin_bit_cast(int, e0), __builtin_bit_cast(int, e1), 0x118, 0xf, 0xc, false)) * f8;
                q[j] = cvt_rpi(__builtin_amdgcn_fmed3f(v, -8388607.f, 8388607.f));
            }
            GAS unsigned char* ab = (GAS unsigned char*)(p->ws + OFF_ACC + (size_t)s * 32768);
#pragma unroll
            for (int j = 0; j < 2; ++j) {
                const u32 lo = ((u32)q[2 * j + 1] << 6) | 1u, hi = ((u32)q[2 * j] << 3) + (u32)(q[2 * j + 1] >> 26);
                __hip_atomic_fetch_add((GAS u64*)(ab + acc_off + j * 1024), (u64)lo | ((u64)hi << 32), RLX_AGENT);
            }
            SB();
            t_pub = clk();
            zc = g >= 2 ? zd[1] : zd[0];
        } else {
            MFMA_DONE2(az[0], az[1]);
            float r[4];
#pragma unroll
            for (int j = 0; j < 4; ++j) r[j] = xsum32(az[0][j]);
            zc = g >= 2 ? (u32x2){pk(z1a, z1b), pk(z1c, z1d)} : (u32x2){pk(r[0], r[1]), pk(r[2], r[3])};
        }
        STAMP(4);
        __builtin_amdgcn_raw_buffer_store_b64(zc, rws, z_lane, z_s + s * 262144, 16);
        const u32 so = (u32)(s + 1) * NH;
        if (!LAST) {
            if (poll) { fseen = have(fnext); if (fseen <= s + 1) wait_proj(s + 1); }
            pk0 = __builtin_amdgcn_raw_buffer_load_b128(rws, t16, kx_off + so * 8192, 16);
            pk1 = __builtin_amdgcn_raw_buffer_load_b128(rws, t16, kx_off + 4096 + so * 8192, 16);
            pq = __builtin_amdgcn_raw_buffer_load_b128(rws, t16, qx_off + so * 8192, 16);
#pragma unroll
            for (int t = 0; t < 2; ++t) ux[t] = __builtin_amdgcn_raw_buffer_load_b128(rws, l16 + t * 1024, ux_off + so * 32768, 16);
        }
        SB();
        if (pjob) {
            f32x4 sp[3];
            u32x4 qx[4];
            const f32x4 m0 = *(LAS f32x4*)(lds + mxr_off + 2048), m1 = *(LAS f32x4*)(lds + mxr_off + 3072);
#pragma unroll
            for (int ks = 0; ks < 4; ++ks) qx[ks] = ldsr4(lds + l16 + L_QX + (s & 1) * 4096 + ks * 1024);
            MF32C(sp[0], F[0], qx[0], cneg); MF32C(sp[1], kx[0][0], qx[0], m0); MF32C(sp[2], kx[1][0], qx[0], m1);
#pragma unroll
            for (int ks = 1; ks < 4; ++ks) { MF32(sp[0], F[ks], qx[ks]); MF32(sp[1], kx[0][ks], qx[ks]); MF32(sp[2], kx[1][ks], qx[ks]); }
            asm volatile("s_nop 7" : "+v"(sp[0]), "+v"(sp[1]), "+v"(sp[2]));
            u32 ra[6];
#pragma unroll
            for (int i = 0; i < 6; ++i) ra[i] = pk(sp[i >> 1][2 * (i & 1)], sp[i >> 1][2 * (i & 1) + 1]);
            SB();
#pragma unroll
            for (int i = 0; i < 6; ++i) { sp[i >> 1][2 * (i & 1)] = lo_f(ra[i]); sp[i >> 1][2 * (i & 1) + 1] = hi_f(ra[i]); }
            SB();
            sp[0] *= SM_SCALE; sp[1] *= SM_SCALE; sp[2] *= SM_SCALE;
            SB();
#pragma unroll
            for (int i = 0; i < 6; ++i) ra[i] = pk(sp[i >> 1][2 * (i & 1)], sp[i >> 1][2 * (i & 1) + 1]);
            SB();
#pragma unroll
            for (int i = 0; i < 6; ++i) { sp[i >> 1][2 * (i & 1)] = lo_f(ra[i]); sp[i >> 1][2 * (i & 1) + 1] = hi_f(ra[i]); }
            SB();
            const float nb = -LOG2E * xmax32(xmax16(max12(sp[0], sp[1], sp[2])));
#pragma unroll
            for (int t = 0; t < 3; ++t)
#pragma unroll
                for (int j = 0; j < 4; ++j) sp[t][j] = __builtin_fmaf(sp[t][j], LOG2E, nb);
            SB();
#pragma unroll
            for (int t = 0; t < 3; ++t)
#pragma unroll
                for (int j = 0; j < 4; ++j) sp[t][j] = __builtin_amdgcn_exp2f(sp[t][j]);
            SB();
            const f32x4 s4 = (sp[0] + sp[1]) + sp[2];
            const float sum = (s4[0] + s4[1]) + (s4[2] + s4[3]);
            const float rs = __builtin_amdgcn_rcpf(xsum32(xsum16(sum)));
            sp[0] *= rs; sp[1] *= rs; sp[2] *= rs;
            st16(pack2(sp[1], sp[2]), rws, l16, (u32)OFF_PXF + (h * 2 + c) * 1024 + s * 65536);
            if (g >= 2) __builtin_amdgcn_raw_buffer_store_b64((u32x2){pk(sp[0][0], sp[0][1]), pk(sp[0][2], sp[0][3])}, rws,
                                                             z_lane, (u32)OFF_PMF + ((h >> 2) * 2 + c - 1) * 1024 + s * 16384, 16);
        }
        STAMPC(5);
        if (!LAST) {
            while (clk() - t_pub < GDELAY) __builtin_amdgcn_s_sleep(1);
            u32x4 v[4];
#pragma unroll
            for (int k = 0; k < 4; ++k) v[k] = __builtin_amdgcn_raw_buffer_load_b128(rws, gat_off + (k & 1) * 2048, gat_s + (k >> 1) * 4096 + s * 32768, 16);
            SB();
            { const u32 o = t16 + ((s + 1) & 1) * 8192; *(LAS u32x4*)(lds + L_KX + o) = pk0; *(LAS u32x4*)(lds + L_KX + 4096 + o) = pk1; *(LAS u32x4*)(lds + L_QX + t16 + ((s + 1) & 1) * 4096) = pq; }
            SB();
            u32 x = v[0].x & v[0].z & v[1].x & v[1].z & v[2].x & v[2].z & v[3].x & v[3].z;
            STAMPC(6);
            if (!__all((x & 32u) != 0u)) {
                for (u32 spins = 0;; ++spins) {
                    asm volatile("" ::: "memory");
#pragma unroll
                    for (int k = 0; k < 4; ++k) v[k] = __builtin_amdgcn_raw_buffer_load_b128(rws, gat_off + (k & 1) * 2048, gat_s + (k >> 1) * 4096 + s * 32768, 16);
                    x = v[0].x & v[0].z & v[1].x & v[1].z & v[2].x & v[2].z & v[3].x & v[3].z;
                    if (__all((x & 32u) != 0u)) break;
                    if (spins > SPIN_CAP) { if (lane == 0) __hip_atomic_store((GAS u32*)(p->ws + OFF_CTL + CTL_TMO), 2u, RLX_AGENT); break; }
                }
            }
#pragma unroll
            for (int k = 0; k < 4; ++k) {
                const float a0 = (float)(int)v[k].y * sc_out, b0 = (float)(int)__builtin_amdgcn_alignbit(v[k].y, v[k].x, 3) * sc_out;
                const float a1 = (float)(int)v[k].w * sc_out, b1v = (float)(int)__builtin_amdgcn_alignbit(v[k].w, v[k].z, 3) * sc_out;
                *(LAS u32x2*)(lds + img_w + k * 64) = (u32x2){pk(a0, b0), pk(a1, b1v)};
            }
            STAMPC(7);
        }
    };
    for (int s = 0; s < NSEG - 1; ++s) step(s, std::false_type{});
    step(NSEG - 1, std::true_type{});
    DRAIN_BAR();
    if (tid == 0) __hip_atomic_fetch_add(ctlp(p, CTL_FIN), 1u, RLX_AGENT);
}

DI u32x2 resid(const u32x2& xv, const f32x4& gv, const f32x4& a) {
    const u32 a0 = pk(a[0], a[1]), a1 = pk(a[2], a[3]);
    const u32 b0 = pk(gv[0] * lo_f(a0), gv[1] * hi_f(a0)), b1 = pk(gv[2] * lo_f(a1), gv[3] * hi_f(a1));
    return (u32x2){pk(lo_f(xv.x) + lo_f(b0), hi_f(xv.x) + hi_f(b0)), pk(lo_f(xv.y) + lo_f(b1), hi_f(xv.y) + hi_f(b1))};
}
template <int NCT>
DI void job_py(KP p, int seg, int cq, ldsp lds, int wave) {
    FRESH(p); FRESH(wave);
    const int lane = lane_id();
    const int g = lane >> 4, li = lane & 15;
    constexpr int RD = NCT == 8 ? 5 : 8;
    const rsrc_t rws = mk_rsrc(p->ws, (u32)WS_END);
    const u32 l16 = lane * 16;
    f32x4 acc[NCT][2];
#pragma unroll
    for (int i = 0; i < NCT; ++i) { acc[i][0] = (f32x4){0, 0, 0, 0}; acc[i][1] = (f32x4){0, 0, 0, 0}; }
    u32x4 ru[RD][NCT], rp[RD][2];
    auto fetch_u = [&](int hh) {
        const u32 ub = hh < 8 ? (u32)OFF_UF + ((seg * NH + wave * 8 + hh) * 32 + cq * NCT) * 1024 : (u32)OFF_ZF + ((seg * 8 + wave * 2 + hh - 8) * 32 + cq * NCT) * 1024;
#pragma unroll
        for (int i = 0; i < NCT; ++i) ru[hh % RD][i] = __builtin_amdgcn_raw_buffer_load_b128(rws, l16 + (i & 3) * 1024, ub + (i >> 2) * 4096, LD_AUX);
    };
    auto fetch_p = [&](int hh) {
        const u32 pb = hh < 8 ? (u32)OFF_PXF + (seg * NH + wave * 8 + hh) * 2048 : (u32)OFF_PMF + (seg * 8 + wave * 2 + hh - 8) * 2048;
#pragma unroll
        for (int i = 0; i < 2; ++i) rp[hh % RD][i] = __builtin_amdgcn_raw_buffer_load_b128(rws, l16 + i * 1024, pb, LD_AUX);
    };
    wg_wait(p, wave, 4u, [&]() { return seg + 1 - (int)ld_dev(ctlp(p, CTL_FQK + (lane & 31) * 128 + 4)); });
#pragma unroll
    for (int hh = 0; hh < RD; ++hh) fetch_u(hh);
    const float* mod = (const float*)(p->ws + OFF_MOD);
    constexpr int NQ = NCT / 4;
    const int ct0 = NCT == 8 ? 2 * wave : 2 * (wave >> 1), qt0 = NCT == 8 ? 0 : (wave & 1);
    u32x2 xv[NQ][2]; f32x4 gv[2];
#pragma unroll
    for (int i = 0; i < 2 * NQ; ++i) xv[i >> 1][i & 1] = *(const GAS u32x2*)(p->x + (size_t)(seg * 32 + (qt0 + (i >> 1)) * 16 + li) * DM + (cq * NCT + ct0 + (i & 1)) * 16 + 4 * g);
#pragma unroll
    for (int i = 0; i < 2; ++i) gv[i] = *(const GAS f32x4*)(mod + 2 * DM + (cq * NCT + ct0 + i) * 16 + 4 * g);
    SB();
    wg_wait(p, wave, 5u, [&]() { const int pr = (int)ld_dev(ctlp(p, CTL_PROG + (blockIdx.x & 7) * 128)); return seg < NSEG - 2 || pr < NSEG - 1 ? seg + 2 - pr : (ld_dev(ctlp(p, CTL_FIN)) >= 128u ? 0 : 1); });
    JLOG(0);
#pragma unroll
    for (int hh = 0; hh < RD; ++hh) fetch_p(hh);
#pragma unroll
    for (int hh = 0; hh < 10; ++hh) {
        SB();
#pragma unroll
        for (int ct = 0; ct < NCT; ++ct) { acc[ct][0] = mfma32(as_bf(ru[hh % RD][ct]), as_bf(rp[hh % RD][0]), acc[ct][0]); acc[ct][1] = mfma32(as_bf(ru[hh % RD][ct]), as_bf(rp[hh % RD][1]), acc[ct][1]); }
        SB();
        if (hh + RD < 10) { fetch_p(hh + RD); fetch_u(hh + RD); }
    }
    SB(); JLOG(1);
#pragma unroll
    for (int ct = 0; ct < NCT; ++ct)
#pragma unroll
        for (int qt = 0; qt < 2; ++qt) *(LAS f32x4*)(lds + ((wave * NCT + ct) * 2 + qt) * 1024 + l16) = acc[ct][qt];
    BAR();
#pragma unroll
    for (int q = 0; q < NQ; ++q) {
        u32x2 r[2];
#pragma unroll
        for (int i = 0; i < 2; ++i) {
            f32x4 a = *(LAS f32x4*)(lds + ((ct0 + i) * 2 + qt0 + q) * 1024 + l16);
#pragma unroll
            for (int w = 1; w < 4; ++w) a += *(LAS f32x4*)(lds + ((w * NCT + ct0 + i) * 2 + qt0 + q) * 1024 + l16);
            r[i] = resid(xv[q][i], gv[i], a);
        }
        st16(cat(r[0], r[1]), rws, l16, (u32)OFF_X2 + ((seg * 2 + qt0 + q) * 16 + (cq * NCT + ct0) / 2) * 1024);
    }
    u32x4 lg[8], lsft[8];
#pragma unroll
    for (int i = 0; i < 8; ++i) { lg[i] = gld4(p->ws + OFF_LN2 + (((wave >> 1) * 8 + i) * 4 + g) * 16); lsft[i] = gld4(p->ws + OFF_LN2 + 1024 + (((wave >> 1) * 8 + i) * 4 + g) * 16); }
    JLOG(2);
    DRAIN_BAR();
    JLOG(3);
    if (wave == 0 && lane_id() == 0) *(LAS u32*)(lds + L_CTL) = __hip_atomic_fetch_add(ctlp(p, CTL_CPY + seg * 128), 1u, RLX_AGENT);
    BAR();
    JLOG(4);
    if (*(LAS u32*)(lds + L_CTL) == 32 / NCT - 1) {
        const u32 fo = ((seg * 2 + (wave & 1)) * 16 + (wave >> 1) * 8) * 1024;
        u32x4 xr[8];
#pragma unroll
        for (int i = 0; i < 8; ++i) xr[i] = __builtin_amdgcn_raw_buffer_load_b128(rws, l16 + (i & 3) * 1024, (u32)OFF_X2 + fo + (i >> 2) * 4096, LD_AUX);
        f32x2 x[32], s2 = {0, 0};
#pragma unroll
        for (int i = 0; i < 32; ++i) { x[i] = (f32x2){lo_f(xr[i >> 2][i & 3]), hi_f(xr[i >> 2][i & 3])}; s2 += x[i]; }
        const float mh = xsum32(xsum16(s2.x + s2.y)) * (1.f / 256);
        f32x2 q2 = {0, 0};
#pragma unroll
        for (int i = 0; i < 32; ++i) { const f32x2 d = x[i] - mh; q2 += d * d; }
        const float qh = xsum32(xsum16(q2.x + q2.y));
        *(LAS f32x2*)(lds + wave * 128 + li * 8) = (f32x2){mh, qh};
        BAR();
        const f32x2 o = *(LAS f32x2*)(lds + (wave ^ 2) * 128 + li * 8);
        const float mb = rbf(0.5f * (mh + o.x)), var = (qh + o.y + 128.f * (mh - o.x) * (mh - o.x)) * (1.f / DM);
        const float rs = __builtin_amdgcn_rcpf(rbf(__builtin_amdgcn_sqrtf(rbf(rbf(var) + 1e-5f))));
#pragma unroll
        for (int i = 0; i < 32; ++i) {
            const u32 gg = lg[i >> 2][i & 3], ss = lsft[i >> 2][i & 3];
            u32 t = pk(x[i].x - mb, x[i].y - mb);
            t = pk(lo_f(t) * rs, hi_f(t) * rs);
            t = pk(lo_f(t) * lo_f(gg), hi_f(t) * hi_f(gg));
            xr[i >> 2][i & 3] = pk(lo_f(t) + lo_f(ss), hi_f(t) + hi_f(ss));
        }
#pragma unroll
        for (int i = 0; i < 8; ++i) st16(xr[i], rws, l16 + (i & 3) * 1024, (u32)OFF_H2F + fo + (i >> 2) * 4096);
        DRAIN_BAR();
        if (wave == 0 && lane_id() == 0) __hip_atomic_store(ctlp(p, CTL_H2 + seg * 128), 1u, RLX_AGENT);
    }
    BAR();
}

DI float swiglu(float g, float u) { const float gv = rbf(g); return rbf(gv * __builtin_amdgcn_rcpf(1.f + __builtin_amdgcn_exp2f(-LOG2E * gv))) * rbf(u); }
DI u32x4 swiglu8(const f32x4& g0, const f32x4& g1, const f32x4& u0, const f32x4& u1) {
    f32x4 t0, t1;
#pragma unroll
    for (int j = 0; j < 4; ++j) { t0[j] = swiglu(g0[j], u0[j]); t1[j] = swiglu(g1[j], u1[j]); }
    return pack2(t0, t1);
}
DI void role_ffn1(KP p, int set, int nc, ldsp lds, int wave) {
    FRESH(p); FRESH(wave);
    const int lane = lane_id();
    const TrB trb = make_trb(lane);
    const rsrc_t rws = mk_rsrc(p->ws, (u32)WS_END);
    const u32 lw = (u32)(size_t)lds + wave * 8192, l16 = lane * 16;
    u32x4 w[64];
    {
        const CV cv = make_cv(lane, wave, FF * 2, true);
        auto ga = [&](ldsp b, int i) { return wfrag(b + 512 * wave, trb, i >> 1, i & 1); };
        load_w(w, mk_rsrc(p->wg, DM * FF * 2), cv, nc * 256, 128 * FF * 2, mk_rsrc(p->wu, DM * FF * 2), cv, nc * 256, 128 * FF * 2, lds, lw, ga, ga);
    }
    for (int s = set; s < NSEG; s += 2) {
        LOGT(s * 32 + nc, 0);
        wg_wait(p, wave, 6u, [&]() { return 1 - (int)ld_dev(ctlp(p, CTL_H2 + s * 128)); });
        LOGT(s * 32 + nc, 1);
        u32x4 hx[8];
#pragma unroll
        for (int i = 0; i < 8; ++i) hx[i] = __builtin_amdgcn_raw_buffer_load_b128(rws, l16 + (i & 3) * 1024, (u32)OFF_H2F + s * 32768 + wave * 8192 + (i >> 2) * 4096, LD_AUX);
#pragma unroll
        for (int i = 0; i < 8; ++i) *(LAS u32x4*)(lds + wave * 8192 + i * 1024 + l16) = hx[i];
        BAR();
        ldsp hb = lds + l16;
        f32x4 acc[2][4];
#pragma unroll
        for (int i = 0; i < 8; ++i) acc[i >> 2][i & 3] = (f32x4){0, 0, 0, 0};
        u32x4 hf[4][2];
#pragma unroll
        for (int ks = 0; ks < 3; ++ks) { hf[ks][0] = ldsr4(hb + ks * 1024); hf[ks][1] = ldsr4(hb + 16384 + ks * 1024); }
#pragma unroll
        for (int ks = 0; ks < 16; ++ks) {
            SB();
            if (ks + 3 < 16) { hf[(ks + 3) & 3][0] = ldsr4(hb + (ks + 3) * 1024); hf[(ks + 3) & 3][1] = ldsr4(hb + 16384 + (ks + 3) * 1024); }
            SB();
#pragma unroll
            for (int t = 0; t < 4; ++t)
#pragma unroll
                for (int tt = 0; tt < 2; ++tt) acc[tt][t] = mfma32(as_bf(w[32 * (t >> 1) + 2 * ks + (t & 1)]), as_bf(hf[ks & 3][tt]), acc[tt][t]);
        }
#pragma unroll
        for (int tt = 0; tt < 2; ++tt)
            st16(swiglu8(acc[tt][0], acc[tt][1], acc[tt][2], acc[tt][3]), rws, l16, (u32)OFF_TF + ((s * 2 + tt) * 56 + nc * 4 + wave) * 1024);
        DRAIN_BAR();
        if (wave == 0 && lane == 0) __hip_atomic_fetch_add(ctlp(p, CTL_CT + s * 128), 1u, RLX_AGENT);
        LOGT(s * 32 + nc, 2);
    }
}

DI void role_ffn2(KP p, int set, int cc, ldsp lds, int wave) {
    FRESH(p); FRESH(wave);
    const int lane = lane_id();
    const int g = lane >> 4, li = lane & 15;
    const TrB trb = make_trb(lane);
    const rsrc_t rws = mk_rsrc(p->ws, (u32)WS_END), rd = mk_rsrc(p->wd, DM * FF * 2);
    const u32 lw = (u32)(size_t)lds + wave * 8192, l16 = lane * 16;
    u32x4 w[56];
    {
        const CV cv = make_cv(lane, wave, 1024, true);
        sfor<2>([&](auto rc) {
            constexpr int nb = rc.value == 0 ? 4 : 3;
            sfor<nb * 8>([&](auto ic) {
                constexpr int b = ic.value >> 3, i = ic.value & 7;
                const u32 so = (rc.value * 8 + b * 2 + (i & 1)) * 131072 + cc * 128, vo = cv.v[i >> 1], la = lw;
                const rsrc_t r = rd;
                DMA16(r, vo, so, la, b * 32768 + i * 1024, 0);
            });
            wait_v<0>(); BAR();
#pragma unroll
            for (int i = 0; i < nb * 8; ++i) w[rc.value * 32 + i] = wfrag(lds + (i >> 3) * 32768 + wave * 8192, trb, 0, i & 7);
            BAR();
        });
    }
    const int col = cc * 64 + (wave >> 1) * 32 + 4 * g, tq = wave & 1;
    const f32x4 gv0 = *(const GAS f32x4*)((const float*)(p->ws + OFF_MOD) + 5 * DM + col), gv1 = *(const GAS f32x4*)((const float*)(p->ws + OFF_MOD) + 5 * DM + col + 16);
    for (int s = set; s < NSEG; s += 2) {
        LOGT(s * 32 + 16 + cc, 0);
        wg_wait(p, wave, 9u, [&]() { return ld_dev(ctlp(p, CTL_CT + s * 128)) >= 14u ? 0 : 1; });
        LOGT(s * 32 + 16 + cc, 1);
        u32x4 tf[28];
#pragma unroll
        for (int i = 0; i < 28; ++i) tf[i] = __builtin_amdgcn_raw_buffer_load_b128(rws, l16, (u32)OFF_TF + ((s * 2 + (i & 1)) * 56 + (i >> 1) * 4 + wave) * 1024, LD_AUX);
        const u32x4 xv = __builtin_amdgcn_raw_buffer_load_b128(rws, l16, (u32)OFF_X2 + ((s * 2 + tq) * 16 + cc * 2 + (wave >> 1)) * 1024, LD_AUX);
        SB();
        f32x4 acc[4][2];
#pragma unroll
        for (int i = 0; i < 8; ++i) acc[i >> 1][i & 1] = (f32x4){0, 0, 0, 0};
#pragma unroll
        for (int kc = 0; kc < 14; ++kc) {
#pragma unroll
            for (int i = 0; i < 8; ++i) acc[i >> 1][i & 1] = mfma32(as_bf(w[4 * kc + (i >> 1)]), as_bf(tf[2 * kc + (i & 1)]), acc[i >> 1][i & 1]);
            SB();
        }
#pragma unroll
        for (int i = 0; i < 8; ++i) *(LAS f32x4*)(lds + (wave * 8 + i) * 1024 + l16) = acc[i >> 1][i & 1];
        BAR();
#pragma unroll
        for (int i = 0; i < 2; ++i) {
            const int t = ((wave >> 1) * 2 + i) * 2 + tq;
            f32x4 a = *(LAS f32x4*)(lds + t * 1024 + l16);
#pragma unroll
            for (int ww = 1; ww < 4; ++ww) a += *(LAS f32x4*)(lds + (ww * 8 + t) * 1024 + l16);
            *(GAS u32x2*)(p->out + (size_t)(s * 32 + tq * 16 + li) * DM + col + 16 * i) = resid(i ? (u32x2){xv.z, xv.w} : (u32x2){xv.x, xv.y}, i ? gv1 : gv0, a);
        }
        LOGT(s * 32 + 16 + cc, 2);
    }
}

__global__ void __launch_bounds__(256, 1) k_main(Params) {
    KP p = (KP)__builtin_amdgcn_kernarg_segment_ptr();
    extern __shared__ __attribute__((aligned(16))) unsigned char smem[];
    ldsp lds = (ldsp)smem;
    const int x = blockIdx.x & 7, y = blockIdx.x >> 3, wave = __builtin_amdgcn_readfirstlane(threadIdx.x >> 6);
    LOGT(2048 + blockIdx.x, 0);
    if (y < 16) chain_role(p, x * 4 + (y >> 2), y & 3, lds, wave);
    else if (y < 24) {
        if (y < 20) role_proj<0>(p, x * 4 + (y & 3), lds, wave); else role_proj<1>(p, x * 4 + (y & 3), lds, wave);
        LOGT(2048 + blockIdx.x, 1);
        for (int s = PY_SPLIT + (y & 7); s < NSEG; s += 8) { LOGT(s * 32 + 24 + x, 0); JSET(s * 32 + 24 + x); job_py<4>(p, s, x, lds, wave); LOGT(s * 32 + 24 + x, 2); }
    } else {
        const int id = (y - 24) * 8 + x;
        if (id < 28) role_ffn1(p, id >= 14, id >= 14 ? id - 14 : id, lds, wave);
        else if (id < 44) role_ffn2(p, (id - 28) >> 3, (id - 28) & 7, lds, wave);
        else for (int s = (id - 44) >> 2; s < PY_SPLIT; s += 5) { LOGT(s * 32 + 24 + (id & 3), 0); JSET(s * 32 + 24 + (id & 3)); job_py<8>(p, s, id & 3, lds, wave); LOGT(s * 32 + 24 + (id & 3), 2); }
    }
    LOGT(2048 + blockIdx.x, 2);
}
}

extern "C" void kernel_launch(void* const* d_in, const int* in_sizes, int n_in, void* d_out, int out_size, void* d_ws, size_t ws_size, hipStream_t stream) {
    Params p;
    p.x = (const u16*)d_in[0]; p.mem0 = (const u16*)d_in[1]; p.wq = (const u16*)d_in[2]; p.wk = (const u16*)d_in[3]; p.wv = (const u16*)d_in[4]; p.wo = (const u16*)d_in[5];
    p.wg = (const u16*)d_in[8]; p.wu = (const u16*)d_in[9]; p.wd = (const u16*)d_in[10]; p.c = (const u16*)d_in[11]; p.wmod = (const u16*)d_in[12];
    p.out = (u16*)d_out; p.ws = (unsigned char*)d_ws;
    (void)hipFuncSetAttribute((const void*)k_main, hipFuncAttributeMaxDynamicSharedMemorySize, L_END);
    hipLaunchKernelGGL(k_mod, dim3(113), dim3(256), 0, stream, p);
    hipLaunchKernelGGL(k_ln, dim3(256), dim3(256), 0, stream, p);
    hipLaunchKernelGGL(k_main, dim3(256), dim3(256), L_END, stream, p);
}
```

```cpp
#include <hip/hip_runtime.h>
#include <hip/hip_bf16.h>
#include <stdint.h>
#include <type_traits>
#include <utility>
#define GDELAY 1300

namespace {
typedef unsigned short u16;
typedef unsigned int u32;
typedef unsigned long long u64;
typedef __bf16 bf16x2 __attribute__((ext_vector_type(2)));
typedef __bf16 bf16x4 __attribute__((ext_vector_type(4)));
typedef __bf16 bf16x8 __attribute__((ext_vector_type(8)));
typedef __bf16 bf16x16 __attribute__((ext_vector_type(16)));
typedef short s16x4 __attribute__((ext_vector_type(4)));
typedef short s16x8 __attribute__((ext_vector_type(8)));
typedef u16 u16x2 __attribute__((ext_vector_type(2)));
typedef float f32x2 __attribute__((ext_vector_type(2)));
typedef float f32x4 __attribute__((ext_vector_type(4)));
typedef u32 u32x2 __attribute__((ext_vector_type(2)));
typedef u32 u32x4 __attribute__((ext_vector_type(4)));
typedef u32 u32x8 __attribute__((ext_vector_type(8)));
typedef u32 u32x32 __attribute__((ext_vector_type(32)));
#define GAS __attribute__((address_space(1)))
#define LAS __attribute__((address_space(3)))
#define DI __device__ __forceinline__
#define RLX_AGENT __ATOMIC_RELAXED, __HIP_MEMORY_SCOPE_AGENT
typedef LAS unsigned char* ldsp;

#define ST_AUX 16
#define LD_AUX 16
#define LD_SC " sc1"
constexpr float SM_SCALE = 0.08838834764831845f, LOG2E = 1.4426950408889634f;
constexpr int DM = 512, NH = 32, NSEG = 64, FF = 1792;
constexpr size_t MB = 1u << 20;
constexpr size_t OFF_CTL = 0, OFF_MOD = 65536  , OFF_LNP = 81920  , OFF_STAT = 262144  , OFF_ACC = 1 * MB, OFF_H1F = 4 * MB, OFF_X2 = 6 * MB, OFF_H2F = 8 * MB, OFF_PXF = 10 * MB,
                 OFF_PMF = 14 * MB, OFF_TF = 16 * MB, OFF_QF = 24 * MB, OFF_KF = 40 * MB, OFF_ZF = 56 * MB, OFF_UF = 72 * MB, WS_END = 136 * MB;
constexpr u32 CTL_BMX = 0;
constexpr u32 CTL_TMO = 1024;
constexpr u32 CTL_EP = 1280;
constexpr u32 CTL_MODF = 2048;
constexpr u32 CTL_H1 = 2560;
constexpr u32 CTL_FIN = 3072;
constexpr u32 CTL_PROG = 4096;
constexpr u32 CTL_FQK = 8192;
constexpr u32 CTL_H2 = 24576;
constexpr u32 CTL_CT = 32768;
constexpr u32 SPIN_CAP = 1u << 17;
constexpr int PY_SPLIT = 44;

struct Params {
    const u16 *x, *mem0, *wq, *wk, *wv, *wo, *wg, *wu, *wd, *c, *wmod;
    u16* out;
    unsigned char* ws;
};

typedef const __attribute__((address_space(4))) Params* KP;
#define FRESH(p) asm volatile("" : "+s"(p))
template <class F, int... I> DI void sfor_(F&& f, std::integer_sequence<int, I...>) { (f(std::integral_constant<int, I>{}), ...); }
template <int N, class F> DI void sfor(F&& f) { sfor_(f, std::make_integer_sequence<int, N>{}); }
DI float lo_f(u32 v) { return __builtin_bit_cast(float, v << 16); }
DI float hi_f(u32 v) { return __builtin_bit_cast(float, v & 0xffff0000u); }
DI u32 pk(float a, float b) { f32x2 f = {a, b}; return __builtin_bit_cast(u32, __builtin_convertvector(f, bf16x2)); }
DI float rbf(float a) { return lo_f(pk(a, a)); }
DI bf16x8 as_bf(u32x4 v) { return __builtin_bit_cast(bf16x8, v); }
DI f32x4 mfma32(bf16x8 a, bf16x8 b, f32x4 c) { return __builtin_amdgcn_mfma_f32_16x16x32_bf16(a, b, c, 0, 0, 0); }
DI f32x4 mfma16(u32x2 a, u32x2 b, f32x4 c) { return __builtin_amdgcn_mfma_f32_16x16x16bf16_1k(__builtin_bit_cast(s16x4, a), __builtin_bit_cast(s16x4, b), c, 0, 0, 0); }
DI f32x4 smfma(bf16x8 a, bf16x16 b, f32x4 c, int idx) { return __builtin_amdgcn_smfmac_f32_16x16x64_bf16(a, b, c, idx, 0, 0); }
#define SMF_A(acc, a, b) asm volatile("v_smfmac_f32_16x16x64_bf16 %0, %1, %2, %3" : "+v"(acc) : "v"(a), "a"(b), "v"(idx))
#define SMF_V(acc, a, b) asm volatile("v_smfmac_f32_16x16x64_bf16 %0, %1, %2, %3" : "+v"(acc) : "v"(a), "v"(b), "v"(idx))
#define SMF_AM(acc, a, b, m, x) do { u32 t_; asm volatile("v_smfmac_f32_16x16x64_bf16 %0, %3, %4, %5\n\tv_and_b32 %2, 0x7fff7fff, %6\n\tv_pk_max_u16 %1, %1, %2" : "+v"(acc), "+v"(m), "=&v"(t_) : "v"(a), "a"(b), "v"(idx), "v"(x)); } while (0)
#define MF32(acc, a, b) asm volatile("v_mfma_f32_16x16x32_bf16 %0, %1, %2, %0" : "+v"(acc) : "v"(a), "v"(b))
#define MF32Z(acc, a, b) asm volatile("v_mfma_f32_16x16x32_bf16 %0, %1, %2, 0" : "=&v"(acc) : "v"(a), "v"(b))
#define MFA(acc, a, b) asm volatile("v_mfma_f32_16x16x32_bf16 %0, %1, %2, %0" : "+v"(acc) : "a"(a), "v"(b))
#define MFAZ(acc, a, b) asm volatile("v_mfma_f32_16x16x32_bf16 %0, %1, %2, 0" : "=&v"(acc) : "a"(a), "v"(b))
#define MFB(acc, a, b) asm volatile("v_mfma_f32_16x16x32_bf16 %0, %1, %2, %0" : "+v"(acc) : "v"(a), "a"(b))
#define MFBZ(acc, a, b) asm volatile("v_mfma_f32_16x16x32_bf16 %0, %1, %2, 0" : "=&v"(acc) : "v"(a), "a"(b))
#define MF32ZN(acc, a, b) asm volatile("s_nop 1\n\tv_mfma_f32_16x16x32_bf16 %0, %1, %2, 0" : "=&v"(acc) : "v"(a), "v"(b))
#define MF16N(acc, a, b) asm volatile("s_nop 1\n\tv_mfma_f32_16x16x16_bf16 %0, %1, %2, %0" : "+v"(acc) : "v"(a), "v"(b))
DI float vmax(float a, float b) { float r; asm("v_max_f32 %0, %1, %2" : "=v"(r) : "v"(a), "v"(b)); return r; }
DI float vmax3(float a, float b, float c) { float r; asm("v_max3_f32 %0, %1, %2, %3" : "=v"(r) : "v"(a), "v"(b), "v"(c)); return r; }
#define SMF4(q0, k0, q1, k1, a, wq0, wk0, wq1, wk1) asm volatile( \
    "v_smfmac_f32_16x16x64_bf16 %0, %4, %5, %9\n\tv_smfmac_f32_16x16x64_bf16 %1, %4, %6, %9\n\t" \
    "v_smfmac_f32_16x16x64_bf16 %2, %4, %7, %9\n\tv_smfmac_f32_16x16x64_bf16 %3, %4, %8, %9" \
    : "+v"(q0), "+v"(k0), "+v"(q1), "+v"(k1) : "v"(a), "a"(wq0), "a"(wk0), "a"(wq1), "a"(wk1), "v"(idx))
#define SMF4_M(q0, k0, q1, k1, a, wq0, wk0, wq1, wk1, m, x0, x1) do { u32 t_; asm volatile( \
    "v_smfmac_f32_16x16x64_bf16 %0, %6, %7, %11\n\tv_and_b32 %5, 0x7fff7fff, %12\n\t" \
    "v_smfmac_f32_16x16x64_bf16 %1, %6, %8, %11\n\tv_pk_max_u16 %4, %4, %5\n\t" \
    "v_smfmac_f32_16x16x64_bf16 %2, %6, %9, %11\n\tv_and_b32 %5, 0x7fff7fff, %13\n\t" \
    "v_smfmac_f32_16x16x64_bf16 %3, %6, %10, %11\n\tv_pk_max_u16 %4, %4, %5" \
    : "+v"(q0), "+v"(k0), "+v"(q1), "+v"(k1), "+v"(m), "=&v"(t_) : "v"(a), "a"(wq0), "a"(wk0), "a"(wq1), "a"(wk1), "v"(idx), "v"(x0), "v"(x1)); } while (0)
#define MF16Z(acc, a, b) asm volatile("v_mfma_f32_16x16x16_bf16 %0, %1, %2, 0" : "=&v"(acc) : "v"(a), "v"(b))
#define MF32N(acc, a, b) asm volatile("s_nop 1\n\tv_mfma_f32_16x16x32_bf16 %0, %1, %2, %0" : "+v"(acc) : "v"(a), "v"(b))
#define MF16(acc, a, b) asm volatile("v_mfma_f32_16x16x16_bf16 %0, %1, %2, %0" : "+v"(acc) : "v"(a), "v"(b))
#define MF32C(acc, a, b, c) asm volatile("v_mfma_f32_16x16x32_bf16 %0, %1, %2, %3" : "=&v"(acc) : "v"(a), "v"(b), "v"(c))
DI float max12(const f32x4& a, const f32x4& b, const f32x4& c) {
    float t0, t1, t2;
    asm("v_max3_f32 %0, %3, %4, %5\n\tv_max3_f32 %1, %6, %7, %8\n\tv_max3_f32 %2, %9, %10, %11\n\tv_max3_f32 %0, %0, %12, %13\n\tv_max3_f32 %1, %1, %2, %14\n\tv_max_f32 %0, %0, %1"
        : "=&v"(t0), "=&v"(t1), "=&v"(t2) : "v"(a[0]), "v"(a[1]), "v"(a[2]), "v"(a[3]), "v"(b[0]), "v"(b[1]), "v"(b[2]), "v"(b[3]), "v"(c[0]), "v"(c[1]), "v"(c[2]), "v"(c[3]));
    return t0;
}
DI int cvt_rpi(float v) { int r; asm("v_cvt_rpi_i32_f32 %0, %1" : "=v"(r) : "v"(v)); return r; }
#define ZERO4(acc) asm volatile("v_mov_b32 %0, 0\n\tv_mov_b32 %1, 0\n\tv_mov_b32 %2, 0\n\tv_mov_b32 %3, 0" : "=v"(acc[0]), "=v"(acc[1]), "=v"(acc[2]), "=v"(acc[3]))
#define MFMA_DONE2(a, b) asm volatile("s_nop 7" : "+v"(a), "+v"(b))
DI u32x2 trd(ldsp p) { return __builtin_bit_cast(u32x2, __builtin_amdgcn_ds_read_tr16_b64_v4i16((LAS s16x4*)p)); }
DI u32x4 cat(u32x2 a, u32x2 b) { return (u32x4){a.x, a.y, b.x, b.y}; }
DI u32x4 ldsr4(ldsp p) { return *(LAS u32x4*)p; }
DI bf16x16 ldsr8(ldsp p) { const u32x4 a = ldsr4(p), b = ldsr4(p + 1024); return __builtin_bit_cast(bf16x16, (u32x8){a.x, a.y, a.z, a.w, b.x, b.y, b.z, b.w}); }
DI u64 clk() { u64 t; asm volatile("s_memtime %0\n\ts_waitcnt lgkmcnt(0)" : "=s"(t) :: "memory"); return t; }
#define SB() __builtin_amdgcn_sched_barrier(0)
DI u32x4 gld4(const void* p) { return *(const GAS u32x4*)p; }
DI void gst4(void* p, u32x4 v) { *(GAS u32x4*)p = v; }
#define WAIT_V(n) asm volatile("s_waitcnt vmcnt(" #n ")" ::: "memory")
#define WAIT_L0() asm volatile("s_waitcnt lgkmcnt(0)" ::: "memory")
#define BAR() do { asm volatile("s_waitcnt lgkmcnt(0)" ::: "memory"); __builtin_amdgcn_s_barrier(); asm volatile("" ::: "memory"); } while (0)
DI float swap32_sum(float a, float b) {
    auto r = __builtin_amdgcn_permlane32_swap(__builtin_bit_cast(u32, a), __builtin_bit_cast(u32, b), false, false);
    const u32 r0 = r[0], r1 = r[1];
    return __builtin_bit_cast(float, r0) + __builtin_bit_cast(float, r1);
}
DI u32 opaque(u32 v) { asm("" : "+v"(v)); return v; }
DI float rfl_f(float v) { return __builtin_bit_cast(float, __builtin_amdgcn_readfirstlane(__builtin_bit_cast(int, v))); }
DI int lane_id() { return (int)opaque(__builtin_amdgcn_mbcnt_hi(~0u, __builtin_amdgcn_mbcnt_lo(~0u, opaque(0u)))); }
DI f32x2 sw16(float v) { const u32 a = __builtin_bit_cast(u32, v); auto r = __builtin_amdgcn_permlane16_swap(a, opaque(a), false, false); const u32 r0 = r[0], r1 = r[1]; return (f32x2){__builtin_bit_cast(float, r0), __builtin_bit_cast(float, r1)}; }
DI f32x2 sw32(float v) { const u32 a = __builtin_bit_cast(u32, v); auto r = __builtin_amdgcn_permlane32_swap(a, opaque(a), false, false); const u32 r0 = r[0], r1 = r[1]; return (f32x2){__builtin_bit_cast(float, r0), __builtin_bit_cast(float, r1)}; }
DI float xmax16(float v) { const f32x2 r = sw16(v); return vmax(r.x, r.y); }
DI float xmax32(float v) { const f32x2 r = sw32(v); return vmax(r.x, r.y); }
DI float xsum16(float v) { const f32x2 r = sw16(v); return r.x + r.y; }
DI float xsum32(float v) { const f32x2 r = sw32(v); return r.x + r.y; }
template <int CTRL> DI u32 dpp(u32 v) { return (u32)__builtin_amdgcn_update_dpp(0, (int)v, CTRL, 0xf, 0xf, true); }
template <int CTRL> DI float dppf(float old, float v) { return __builtin_bit_cast(float, __builtin_amdgcn_update_dpp(__builtin_bit_cast(int, old), __builtin_bit_cast(int, v), CTRL, 0xf, 0xf, false)); }
template <int CTRL> DI float dppf(float v) { return __builtin_bit_cast(float, dpp<CTRL>(__builtin_bit_cast(u32, v))); }
DI float wave_sum(float v) {
    v += dppf<0xB1>(v);
    v += dppf<0x4E>(v);
    v += dppf<0x124>(v);
    v += dppf<0x128>(v);
    v = xsum16(v); v = xsum32(v);
    return v;
}
DI float wave_max(float v) {
    v = fmaxf(v, dppf<0xB1>(v)); v = fmaxf(v, dppf<0x4E>(v)); v = fmaxf(v, dppf<0x124>(v)); v = fmaxf(v, dppf<0x128>(v));
    v = xmax16(v); v = xmax32(v);
    return v;
}

DI u32 wave_umax(u32 v) {
    v = max(v, dpp<0xB1>(v)); v = max(v, dpp<0x4E>(v)); v = max(v, dpp<0x124>(v)); v = max(v, dpp<0x128>(v));
    { auto r = __builtin_amdgcn_permlane16_swap(v, opaque(v), false, false); const u32 a = r[0], b = r[1]; v = max(a, b); }
    { auto r = __builtin_amdgcn_permlane32_swap(v, opaque(v), false, false); const u32 a = r[0], b = r[1]; v = max(a, b); }
    return v;
}

typedef __amdgpu_buffer_rsrc_t rsrc_t;
DI rsrc_t mk_rsrc(const void* p, u32 bytes) { return __builtin_amdgcn_make_buffer_rsrc((void*)p, 0, (int)bytes, 0x00020000); }
DI void st16(u32x4 v, rsrc_t r, u32 voff, u32 soff) { __builtin_amdgcn_raw_buffer_store_b128(v, r, voff, soff, ST_AUX); asm volatile("s_nop 1" :: "v"(v) : "memory"); }
#define DMA16(r, voff, soff, ldsa, LO, GO) asm volatile("s_add_u32 m0, %0, %4\n\ts_nop 0\n\tbuffer_load_dwordx4 %1, %2, %3 offen offset:%5 lds" \
    :: "s"(ldsa), "v"(voff), "s"(r), "s"(soff), "n"((LO) - (GO)), "n"(GO) : "memory", "scc")
#define DMA16S(r, voff, soff, ldsa, LO, GO) asm volatile("s_add_u32 m0, %0, %4\n\ts_nop 0\n\tbuffer_load_dwordx4 %1, %2, %3 offen offset:%5 sc1 lds" \
    :: "s"(ldsa), "v"(voff), "s"(r), "s"(soff), "n"((LO) - (GO)), "n"(GO) : "memory", "scc")
DI u32 chunk_voff(int lane, int wave, u32 ldb, bool perm, int rg) {
    const int r7 = (lane & 31) >> 2;
    const int rl = 32 * wave + (perm ? 16 * (r7 >> 2) + (r7 & 3) + 4 * rg : r7 + 8 * rg);
    return rl * ldb + (4 * (lane >> 5) + ((lane & 3) ^ ((2 * rg + (r7 >> 2)) & 3))) * 16;
}
struct CV { u32 v[4]; };
DI CV make_cv(int lane, int wave, u32 ldb, bool perm) { CV c; for (int i = 0; i < 4; ++i) c.v[i] = chunk_voff(lane, wave, ldb, perm, i); return c; }
#define CHUNK_PIECE(r, cv, soff, lw, LO, i) DMA16(r, cv.v[(i) >> 1], soff, lw, (LO) + (i) * 1024, ((i) & 1) * 128)
template <int LO, int I> DI void chunk_piece(rsrc_t r, const CV& cv, u32 soff, u32 lw) { CHUNK_PIECE(r, cv, soff, lw, LO, I); }
#define ALD4(dst, r, voff, soff, GO, AUX) asm volatile("buffer_load_dwordx4 %0, %1, %2, %3 offen offset:%4" AUX : "=v"(dst) : "v"(voff), "s"(r), "s"(soff), "n"(GO) : "memory")
#define ALD4P(dst, REG, r, voff, soff, GO) asm volatile("buffer_load_dwordx4 %0, %1, %2, %3 offen offset:%4" : "={" REG "}"(dst) : "v"(voff), "s"(r), "s"(soff), "n"(GO) : "memory")
#define PIN8A(a, o) asm volatile("" : "+a"(a[o]), "+a"(a[o + 1]), "+a"(a[o + 2]), "+a"(a[o + 3]), "+a"(a[o + 4]), "+a"(a[o + 5]), "+a"(a[o + 6]), "+a"(a[o + 7]))
#define PIN8(a, o) asm volatile("" : "+v"(a[o]), "+v"(a[o + 1]), "+v"(a[o + 2]), "+v"(a[o + 3]), "+v"(a[o + 4]), "+v"(a[o + 5]), "+v"(a[o + 6]), "+v"(a[o + 7]))
template <int N> DI void wait_v() { asm volatile("s_waitcnt vmcnt(%0)" :: "n"(N) : "memory"); }
#define JLOG(k)
#define JSET(v)
#define LOGT(slot, k)
DI GAS u32* ctlp(KP p, u32 off) { return (GAS u32*)(p->ws + OFF_CTL + off); }
DI u32 ld_dev(GAS u32* q) { return __hip_atomic_load(q, RLX_AGENT); }
DI void put_tag(KP p, u32 off, u32 ep) { __hip_atomic_store(ctlp(p, off), ep, RLX_AGENT); }
DI void put_cnt(KP p, u32 off, u32 n, u32 ep) { __hip_atomic_store((GAS u64*)ctlp(p, off), (u64)n | ((u64)ep << 32), RLX_AGENT); }
DI int get_cnt(KP p, u32 off, u32 ep) { const u64 v = __hip_atomic_load((GAS u64*)ctlp(p, off), RLX_AGENT); return (u32)(v >> 32) == ep ? (int)(u32)v : 0; }
DI int no_tag(KP p, u32 off, u32 ep) { return ld_dev(ctlp(p, off)) != ep; }
template <class F> DI void wg_wait(KP p, int wave, u32 code, F&& left) {
    if (wave == 0) {
        for (u32 spins = 0;; ++spins) {
            const int d = left();
            if (__all(d <= 0)) break;
            if (spins > SPIN_CAP) { __hip_atomic_store(ctlp(p, CTL_TMO), code, RLX_AGENT); break; }
            if (__any(d > 2)) __builtin_amdgcn_s_sleep(40); else __builtin_amdgcn_s_sleep(2);
        }
    }
    BAR();
}
#define DRAIN_BAR() do { wait_v<0>(); BAR(); } while (0)
DI u32 tr_base(int lane, int c1, int a) {
    const int g = lane >> 4, q = (lane & 15) >> 2, p = lane & 3;
    return 2048u * g + 64u * (4 * a + q) + 16u * ((2 * c1 + (p >> 1)) ^ ((2 * g + a) & 3)) + 8u * (p & 1);
}
struct TrB { u32 b[2][2]; };
DI TrB make_trb(int lane) { TrB t; t.b[0][0] = tr_base(lane, 0, 0); t.b[0][1] = tr_base(lane, 0, 1); t.b[1][0] = tr_base(lane, 1, 0); t.b[1][1] = tr_base(lane, 1, 1); return t; }
DI u32x4 wfrag(ldsp buf, const TrB& t, int ks, int ct) {
    const int o = 8192 * ks + 512 * (ct >> 1);
    return cat(trd(buf + t.b[ct & 1][0] + o), trd(buf + t.b[ct & 1][1] + o));
}
DI u32x4 pack2(const f32x4& a, const f32x4& b) { return (u32x4){pk(a[0], a[1]), pk(a[2], a[3]), pk(b[0], b[1]), pk(b[2], b[3])}; }

DI void job_mod(KP p, int b, ldsp lds, int wave, const u32 ep) {
    FRESH(p);
    const int lane = lane_id(), tid = wave * 64 + lane, cg = tid & 7, rg = tid >> 3;
    u32x4 w[16]; u16 cv[16];
#pragma unroll
    for (int i = 0; i < 16; ++i) { w[i] = gld4(p->wmod + (size_t)(rg + 32 * i) * 3072 + b * 64 + cg * 8); cv[i] = p->c[rg + 32 * i]; }
    float acc[8] = {0, 0, 0, 0, 0, 0, 0, 0};
#pragma unroll
    for (int i = 0; i < 16; ++i) {
        const float x = lo_f(cv[i]), sl = rbf(x * __builtin_amdgcn_rcpf(1.f + __builtin_amdgcn_exp2f(-LOG2E * x)));
#pragma unroll
        for (int j = 0; j < 4; ++j) { acc[2 * j] += sl * lo_f(w[i][j]); acc[2 * j + 1] += sl * hi_f(w[i][j]); }
    }
#pragma unroll
    for (int j = 0; j < 8; ++j) *(LAS float*)(lds + (rg * 65 + cg * 8 + j) * 4) = acc[j];
    BAR();
    if (wave == 0) {
        float sum = 0;
#pragma unroll 8
        for (int i = 0; i < 32; ++i) sum += *(LAS float*)(lds + (i * 65 + lane) * 4);
        sum = rbf(sum);
        const int v = b >> 3, d = (b & 7) * 64 + lane;
        __hip_atomic_store((GAS u32*)(p->ws + OFF_MOD) + b * 64 + lane, __builtin_bit_cast(u32, sum), RLX_AGENT);
        const int pos = v < 3 ? d : (d & ~31) + ((d >> 2) & 3) * 8 + ((d >> 4) & 1) * 4 + (d & 3);
        __hip_atomic_store((GAS u16*)(p->ws + OFF_LNP) + v * DM + pos, (u16)pk(v == 1 || v == 4 ? 1.f + sum : sum, 0.f), RLX_AGENT);
        wait_v<0>();
        if (lane == 0) put_tag(p, CTL_MODF + b * 4, ep);
    }
    BAR();
}
DI void ln_norm(u32x4 (&xr)[8], ldsp lds, int wave, int li) {
    f32x2 x[32], s2 = {0, 0}, s3 = {0, 0};
#pragma unroll
    for (int i = 0; i < 32; ++i) { x[i] = (f32x2){lo_f(xr[i >> 2][i & 3]), hi_f(xr[i >> 2][i & 3])}; if (i & 1) s3 += x[i]; else s2 += x[i]; }
    s2 += s3;
    const float mh = xsum32(xsum16(s2.x + s2.y)) * (1.f / 256);
    f32x2 q2 = {0, 0}, q3 = {0, 0};
#pragma unroll
    for (int i = 0; i < 32; ++i) { const f32x2 d = x[i] - mh; if (i & 1) q3 += d * d; else q2 += d * d; }
    q2 += q3;
    const float qh = xsum32(xsum16(q2.x + q2.y));
    *(LAS f32x2*)(lds + wave * 128 + li * 8) = (f32x2){mh, qh};
    BAR();
    const f32x2 o = *(LAS f32x2*)(lds + (wave ^ 2) * 128 + li * 8);
    const float mb = rbf(0.5f * (mh + o.x)), var = (qh + o.y + 128.f * (mh - o.x) * (mh - o.x)) * (1.f / DM);
    const float rs = __builtin_amdgcn_rcpf(rbf(__builtin_amdgcn_sqrtf(rbf(rbf(var) + 1e-5f))));
#pragma unroll
    for (int i = 0; i < 32; ++i) { const u32 t = pk(x[i].x - mb, x[i].y - mb); xr[i >> 2][i & 3] = pk(lo_f(t) * rs, hi_f(t) * rs); }
}
DI void ln_mod(u32x4 (&xr)[8], const u32x4 (&lg)[8], const u32x4 (&ls)[8]) {
#pragma unroll
    for (int i = 0; i < 32; ++i) {
        const u32 n = xr[i >> 2][i & 3], gg = lg[i >> 2][i & 3], ss = ls[i >> 2][i & 3];
        const u32 t = pk(lo_f(n) * lo_f(gg), hi_f(n) * hi_f(gg));
        xr[i >> 2][i & 3] = pk(lo_f(t) + lo_f(ss), hi_f(t) + hi_f(ss));
    }
}
DI void ln_load_x(KP p, u32x4 (&xr)[8], int seg, int wave, int lane) {
#pragma unroll
    for (int i = 0; i < 8; ++i) xr[i] = gld4(p->x + (size_t)(seg * 32 + (wave & 1) * 16 + (lane & 15)) * DM + ((wave >> 1) * 8 + i) * 32 + (lane >> 4) * 8);
}
DI void ln_params(rsrc_t rws, u32x4 (&lg)[8], u32x4 (&ls)[8], int wave, int g) {
#pragma unroll
    for (int i = 0; i < 8; ++i) {
        ls[i] = __builtin_amdgcn_raw_buffer_load_b128(rws, g * 16 + i * 64, (u32)OFF_LNP + (wave >> 1) * 512, LD_AUX);
        lg[i] = __builtin_amdgcn_raw_buffer_load_b128(rws, g * 16 + i * 64, (u32)OFF_LNP + 1024 + (wave >> 1) * 512, LD_AUX);
    }
}
DI void job_ln1(KP p, int seg, ldsp lds, int wave, const u32 ep) {
    const int lane = lane_id(), g = lane >> 4, li = lane & 15;
    const rsrc_t rws = mk_rsrc(p->ws, (u32)WS_END);
    u32x4 xr[8], lg[8], ls[8];
    ln_load_x(p, xr, seg, wave, lane);
    ln_norm(xr, lds, wave, li);
    st16((u32x4){0u, 0u, 0u, 0u}, rws, lane * 16, (u32)OFF_STAT + seg * 4096 + wave * 1024);
    wg_wait(p, wave, 10u, [&]() { return no_tag(p, CTL_MODF + (lane & 15) * 4, ep); });
    LOGT(2560 + blockIdx.x, 1);
    ln_params(rws, lg, ls, wave, g);
    ln_mod(xr, lg, ls);
    LOGT(2560 + blockIdx.x, 2);
#pragma unroll
    for (int i = 0; i < 8; ++i) st16(xr[i], rws, lane * 16 + (i & 3) * 1024, (u32)OFF_H1F + ((seg * 2 + (wave & 1)) * 16 + (wave >> 1) * 8 + (i & 4)) * 1024);
    DRAIN_BAR();
    if (wave == 0 && lane == 0) put_tag(p, CTL_H1 + seg * 4, ep);
}

template <class G1, class G2, class HP, class H0, class H>
DI void load_w(u32x4 (&w)[64], rsrc_t r1, const CV& c1, u32 o1, u32 s1, rsrc_t r2, const CV& c2, u32 o2, u32 s2, ldsp lds, u32 lw, G1&& g1, G2&& g2, HP&& pre, H0&& mid, H&& last) {
    sfor<4>([&](auto cc) { sfor<8>([&](auto pc) { chunk_piece<cc.value * 32768, pc.value>(r1, c1, o1 + cc.value * s1, lw); }); });
    pre();
    sfor<8>([&](auto ic) {
        constexpr int m = ic.value >> 2, cc = ic.value & 3;
        if constexpr (ic.value == 4) mid();
        wait_v<(m == 0 ? 24 : 24 - 8 * cc)>(); BAR();
        if constexpr (ic.value == 7) last();
#pragma unroll
        for (int i = 0; i < 8; ++i) w[32 * m + 8 * cc + i] = m == 0 ? g1(lds + cc * 32768, i) : g2(lds + cc * 32768, i);
        PIN8A(w, 32 * m + 8 * cc);
        BAR();
        if constexpr (m == 0) sfor<8>([&](auto pc) { chunk_piece<cc * 32768, pc.value>(r2, c2, o2 + cc * s2, lw); });
    });
}
template <int NT, class F>
DI void seg_gemm(f32x4 (&acc)[2][NT], const u32x4 (&w)[64], ldsp hb, F&& side) {
    u32x4 hf[4][2];
#pragma unroll
    for (int ks = 0; ks < 3; ++ks) { hf[ks][0] = ldsr4(hb + ks * 1024); hf[ks][1] = ldsr4(hb + 16384 + ks * 1024); }
    sfor<16>([&](auto kc) {
        constexpr int ks = kc.value;
        sfor<2 * NT>([&](auto ic) {
            constexpr int t = ic.value >> 1, tt = ic.value & 1;
            if constexpr (ks == 0) MFAZ(acc[tt][t], w[32 * (t >> 1) + (t & 1)], hf[0][tt]); else MFA(acc[tt][t], w[32 * (t >> 1) + 2 * ks + (t & 1)], hf[ks & 3][tt]);
            if constexpr (ic.value < 2 && ks + 3 < 16) hf[(ks + 3) & 3][ic.value] = ldsr4(hb + ic.value * 16384 + (ks + 3) * 1024);
            if constexpr (ic.value == 2) side(kc);
            SB();
        });
    });
    asm volatile("s_nop 7" ::: "memory");
#pragma unroll
    for (int t = 0; t < NT; ++t) asm volatile("" : "+v"(acc[0][t]), "+v"(acc[1][t]));
}
DI u32 ring_off(int s) { return (s & 3) == 0 ? 131072u : (u32)((s & 3) - 1) * 32768u; }
template <int TYPE>
DI void role_proj(KP p, int h, ldsp lds, int wave, const u32 ep) {
    FRESH(p); FRESH(wave);
    const int lane = lane_id();
    const TrB trb = make_trb(lane);
    const rsrc_t rws = mk_rsrc(p->ws, (u32)WS_END);
    const u32 l0 = (u32)(size_t)lds, lw = l0 + wave * 8192, l16 = lane * 16;
    auto fetch = [&](int s) {
        const u32 so = (u32)OFF_H1F + s * 32768 + wave * 8192, la = lw + ring_off(s);
#pragma unroll
        for (int i = 0; i < 4; ++i) DMA16S(rws, l16, so, la, i * 1024, i * 1024);
#pragma unroll
        for (int i = 0; i < 4; ++i) DMA16S(rws, l16, so + 4096, la, 4096 + i * 1024, i * 1024);
    };
    u32x4 xr[8], lg[8], ls[8];
    ln_load_x(p, xr, 0, wave, lane);
    PIN8(xr, 0);
    auto pre = [&]() { ln_norm(xr, lds + 131072, wave, lane & 15); };
    auto poll = [&](u32 off, u32 code) {
        for (u32 spins = 0; __any(no_tag(p, off, ep)); ++spins) {
            if (spins > SPIN_CAP) { __hip_atomic_store(ctlp(p, CTL_TMO), code, RLX_AGENT); break; }
            __builtin_amdgcn_s_sleep(2);
        }
    };
    auto mid = [&]() {
        poll(CTL_MODF + (lane & 15) * 4, 11u);
#pragma unroll
        for (int i = 0; i < 8; ++i) {
            ALD4(ls[i], rws, (lane >> 4) * 16, (u32)OFF_LNP + (wave >> 1) * 512, i * 64, " sc1");
            ALD4(lg[i], rws, (lane >> 4) * 16, (u32)OFF_LNP + 1024 + (wave >> 1) * 512, i * 64, " sc1");
        }
    };
    auto first = [&]() {
        PIN8(ls, 0); PIN8(lg, 0);
        ln_mod(xr, lg, ls);
#pragma unroll
        for (int i = 0; i < 8; ++i) *(LAS u32x4*)(lds + 131072 + ((wave & 1) * 16 + (wave >> 1) * 8 + i) * 1024 + l16) = xr[i];
    };
    u32x4 w[64];
    {
        const CV ca = make_cv(lane, wave, 8192, false);
        auto ga = [&](ldsp b, int i) { return wfrag(b + 512 * wave, trb, i >> 1, i & 1); };
        if constexpr (TYPE == 0) load_w(w, mk_rsrc(p->wq, 4u << 20), ca, h * 256, 1048576, mk_rsrc(p->wk, 4u << 20), ca, h * 256, 1048576, lds, lw, ga, ga, pre, mid, first);
        else {
            const CV co = make_cv(lane, wave, 1024, true);
            load_w(w, mk_rsrc(p->wv, 4u << 20), ca, h * 256, 1048576, mk_rsrc(p->wo, 4u << 20), co, h * 131072, 256, lds, lw, ga, ga, pre, mid, first);
        }
    }
    LOGT(2560 + blockIdx.x, 0);
    poll(CTL_H1 + (lane & 3) * 4, 16u);
    fetch(1); fetch(2); fetch(3);
    const u32 flag = CTL_FQK + h * 128 + TYPE * 8;
    constexpr u32 L_V = 98304;
    bool allh = false;
    for (int s = 0; s < NSEG; ++s) {
        if (s == 1) wait_v<(TYPE ? 24 : 20)>();
        BAR();
        if (s == 1) LOGT(2560 + blockIdx.x, 1);
        if (s == 10) LOGT(2560 + blockIdx.x, 2);
        const bool more = s > 0 && s + 3 < NSEG;
        for (u32 spins = 0; more && !allh; ++spins) {
            const u64 miss = __ballot(no_tag(p, CTL_H1 + lane * 4, ep));
            allh = miss == 0;
            if (!((miss >> (s + 3)) & 1)) break;
            if (spins > SPIN_CAP) { __hip_atomic_store(ctlp(p, CTL_TMO), 12u, RLX_AGENT); break; }
            __builtin_amdgcn_s_sleep(2);
        }
        const u32 so = (u32)OFF_H1F + (s + 3) * 32768 + wave * 8192, la = lw + ring_off(s + 3);
        f32x4 acc[2][TYPE ? 2 : 4];
        seg_gemm(acc, w, lds + ring_off(s) + l16, [&](auto kc) {
            constexpr int i = kc.value;
            if constexpr (i < 8) { const u32 so2 = so + (i >> 2) * 4096, la2 = la, v = l16; const rsrc_t r = rws; if (more) DMA16S(r, v, so2, la2, i * 1024, (i & 3) * 1024); }
            if constexpr (i == 10) if (s > 0) {
                if (more) wait_v<8>(); else wait_v<0>();
                __builtin_amdgcn_s_barrier();
                if (wave == 0 && lane == 0) put_cnt(p, flag, s, ep);
            }
        });
        if constexpr (TYPE == 0) {
#pragma unroll
            for (int i = 0; i < 4; ++i) st16(pack2(acc[i & 1][(i >> 1) * 2], acc[i & 1][(i >> 1) * 2 + 1]), rws, l16,
                                             (u32)(i >> 1 ? OFF_KF : OFF_QF) + (s * NH + h) * 8192 + ((i & 1) * 4 + wave) * 1024);
        } else {
#pragma unroll
            for (int tt = 0; tt < 2; ++tt) *(LAS u32x4*)(lds + L_V + (tt * 4 + wave) * 1024 + l16) = pack2(acc[tt][0], acc[tt][1]);
            BAR();
            u32x4 vf[8], o;
#pragma unroll
            for (int i = 0; i < 8; ++i) vf[i] = ldsr4(lds + L_V + i * 1024 + l16);
            f32x4 ua[2][2];
            sfor<9>([&](auto tc) {
                constexpr int t = tc.value, tp = t - 1;
                sfor<8>([&](auto ic) {
                    constexpr int i = ic.value, ks = i >> 1, tt = i & 1;
                    if constexpr (t < 8) { if constexpr (ks == 0) MFBZ(ua[t & 1][tt], vf[4 * tt], w[32 + 8 * (t >> 1) + (t & 1)]); else MFB(ua[t & 1][tt], vf[4 * tt + ks], w[32 + 8 * (t >> 1) + 2 * ks + (t & 1)]); }
                    if constexpr (tp >= 0) {
                        if constexpr (i == 3) asm volatile("s_nop %2" : "+v"(ua[tp & 1][0]), "+v"(ua[tp & 1][1]) : "n"(t < 8 ? 3 : 7));
                        if constexpr (i == 4 || i == 5) { o[2 * (i - 4)] = pk(ua[tp & 1][i - 4][0], ua[tp & 1][i - 4][1]); o[2 * (i - 4) + 1] = pk(ua[tp & 1][i - 4][2], ua[tp & 1][i - 4][3]); }
                        if constexpr (i == 6) st16(o, rws, l16 + (tp & 1) * 1024, (u32)OFF_UF + ((s * NH + h) * 32 + 8 * (tp >> 1) + 2 * wave) * 1024);
                    }
                    SB();
                });
            });
        }
    }
    DRAIN_BAR();
    if (wave == 0 && lane == 0) put_cnt(p, flag, NSEG, ep);
    poll(CTL_MODF + min(lane, 47) * 4, 15u);
}

DI int dperm(int pos) { const int B = pos >> 4, tb = (pos >> 3) & 1, g = (pos >> 1) & 3, b = pos & 1; return 32 * (B >> 1) + 16 * tb + 4 * g + 2 * (B & 1) + b; }
DI int posl(int k) { return (k & 48) + 8 * ((k >> 1) & 1) + 2 * ((k >> 2) & 3) + (k & 1); }
constexpr int L_BS = 0, L_IMG = 131072, ROWB = 1088, L_FI = L_IMG + 8 * ROWB, L_SS = L_FI + 4096, L_TAB = L_SS + 4096, L_END = 163840;

#define CHAIN_ASM \
    "s_mov_b64 s[60:61], %[wsp]\n\t" \
    "s_mov_b32 s62, %[wsn]\n\t" \
    "s_mov_b32 s63, 0x20000\n\t" \
    "s_mov_b32 s64, %[kx]\n\t" \
    "s_mov_b32 s65, %[ux]\n\t" \
    "s_mov_b32 s66, %[qx]\n\t" \
    "s_mov_b32 s67, %[zs]\n\t" \
    "s_mov_b32 s68, %[gat]\n\t" \
    "s_mov_b32 s69, %[pxf]\n\t" \
    "s_mov_b32 s70, %[pmf]\n\t" \
    "s_mov_b32 s71, %[zero]\n\t" \
    "s_mov_b32 s72, %[thrm]\n\t" \
    "s_mov_b32 s73, %[eb0]\n\t" \
    "s_mov_b32 s74, %[bmax]\n\t" \
    "s_mov_b32 s75, %[fqk]\n\t" \
    "s_mov_b32 s76, %[fseen]\n\t" \
    "s_and_b32 s41, %[wid], 11\n\t" \
    "s_cmp_eq_u32 s41, 1\n\t" \
    "s_cselect_b32 s77, 1, 0\n\t" \
    "s_cmp_eq_u32 %[wid], 15\n\t" \
    "s_cselect_b64 s[78:79], 0xff, 0\n\t" \
    "s_and_b32 s41, %[wid], 3\n\t" \
    "s_cmp_eq_u32 s41, 2\n\t" \
    "s_cselect_b64 s[80:81], 0xff, 0\n\t" \
    "s_mov_b32 s82, 0x33333333\n\t" \
    "s_mov_b32 s83, 0x33333333\n\t" \
    "s_movk_i32 s99, 0x1000\n\t" \
    "s_mov_b32 s56, 0x3e0293ee\n\t" \
    "s_mov_b32 s58, 0x3db504f3\n\t" \
    "s_mov_b32 s84, 0x3fb8aa3b\n\t" \
    "s_mov_b32 s85, 0x100000\n\t" \
    "s_mov_b32 s98, %[ep]\n\t" \
    "ds_read_b32 v72, %[tab] offset:0\n\t" \
    "ds_read_b32 v73, %[tab] offset:1024\n\t" \
    "ds_read_b32 v74, %[tab] offset:2048\n\t" \
    "ds_read_b32 v75, %[tab] offset:3072\n\t" \
    "ds_read_b32 v76, %[tab] offset:4096\n\t" \
    "ds_read_b32 v77, %[tab] offset:5120\n\t" \
    "ds_read_b32 v78, %[tab] offset:6144\n\t" \
    "ds_read_b32 v79, %[tab] offset:7168\n\t" \
    "ds_read_b32 v80, %[tab] offset:8192\n\t" \
    "ds_read_b32 v81, %[tab] offset:9216\n\t" \
    "ds_read_b32 v82, %[tab] offset:10240\n\t" \
    "ds_read_b32 v83, %[tab] offset:11264\n\t" \
    "ds_read_b32 v84, %[tab] offset:12288\n\t" \
    "ds_read_b32 v85, %[tab] offset:13312\n\t" \
    "s_waitcnt lgkmcnt(0)\n\t" \
    "v_mov_b32 v88, 0x3f803f80\n\t" \
    "v_mov_b32 v89, 0x3f803f80\n\t" \
    "v_mov_b32 v90, 0x3f803f80\n\t" \
    "v_mov_b32 v91, 0x3f803f80\n\t" \
    "v_mov_b32 v92, 0\n\t" \
    "v_mov_b32 v86, 0\n\t" \
    "v_mov_b32 v94, 0\n\t" \
    "buffer_load_dwordx4 v[2:5], off, s[60:63], s75 sc1\n\t" \
    "s_mov_b32 s43, 0\n\t" \
    "s_lshl_b32 s41, s43, 18\n\t" \
    "s_add_u32 s94, s41, s66\n\t" \
    "s_add_u32 s92, s41, s64\n\t" \
    "s_add_u32 s93, s92, 0x1000\n\t" \
    "s_lshl_b32 s41, s43, 20\n\t" \
    "s_add_u32 s95, s41, s65\n\t" \
    "buffer_load_dwordx4 v[144:147], v80, s[60:63], s95 offen sc1\n\t" \
    "buffer_load_dwordx4 v[148:151], v80, s[60:63], s95 offen offset:1024 sc1\n\t" \
    "buffer_load_dwordx4 v[116:119], v81, s[60:63], s92 offen sc1\n\t" \
    "buffer_load_dwordx4 v[120:123], v81, s[60:63], s93 offen sc1\n\t" \
    "buffer_load_dwordx4 v[124:127], v81, s[60:63], s94 offen sc1\n\t" \
    "v_mov_b64 v[200:201], 0\n\t" \
    "v_mov_b64 v[202:203], 0\n\t" \
    "v_mov_b64 v[204:205], 0\n\t" \
    "v_mov_b64 v[206:207], 0\n\t" \
    "v_mov_b64 v[208:209], 0\n\t" \
    "v_mov_b64 v[210:211], 0\n\t" \
    "v_mov_b64 v[212:213], 0\n\t" \
    "v_mov_b64 v[214:215], 0\n\t" \
    "v_mov_b64 v[216:217], 0\n\t" \
    "v_mov_b64 v[218:219], 0\n\t" \
    "v_mov_b64 v[220:221], 0\n\t" \
    "v_mov_b64 v[222:223], 0\n\t" \
    "v_mov_b32 v87, 0\n\t" \
    "v_add_u32 v7, 0x10000, v80\n\t" \
    "s_mov_b32 s40, 0\n\t" \
    "s_mov_b32 s53, 0x10000\n\t" \
    "s_mov_b32 s54, 0\n\t" \
    "s_mov_b32 s52, 0\n\t" \
    "s_movk_i32 s57, 1300\n\t" \
    "s_movk_i32 s86, 0\n\t" \
    "s_mov_b32 s59, 0\n\t" \
    "s_waitcnt lgkmcnt(0)\n\t" \
    "s_barrier\n\t" \
    "s_waitcnt vmcnt(0)\n\t" \
    "v_add_u32 v6, s53, v81\n\t" \
    "ds_write_b128 v6, v[116:119]\n\t" \
    "ds_write_b128 v6, v[120:123] offset:4096\n\t" \
    "ds_write_b128 v6, v[124:127] offset:32768\n\t" \
    "v_mov_b64 v[96:97], v[144:145]\n\t" \
    "v_mov_b64 v[98:99], v[146:147]\n\t" \
    "v_mov_b64 v[100:101], v[148:149]\n\t" \
    "v_mov_b64 v[102:103], v[150:151]\n\t" \
    "s_mov_b32 s43, 1\n\t" \
    "s_lshl_b32 s41, s43, 18\n\t" \
    "s_add_u32 s94, s41, s66\n\t" \
    "s_add_u32 s92, s41, s64\n\t" \
    "s_add_u32 s93, s92, 0x1000\n\t" \
    "s_lshl_b32 s41, s43, 20\n\t" \
    "s_add_u32 s95, s41, s65\n\t" \
    "s_branch ENTRY%=\n\t" \
    ".p2align 6\n\t" \
    "DEC%=:\n\t" \
    "s_waitcnt vmcnt(3)\n\t" \
    "v_cvt_f32_i32 v184, v169\n\t" \
    "v_alignbit_b32 v185, v169, v168, 3\n\t" \
    "v_alignbit_b32 v187, v171, v170, 3\n\t" \
    "v_cvt_f32_i32_e64 v186, v171\n\t" \
    "v_cvt_f32_i32 v185, v185\n\t" \
    "v_cvt_f32_i32 v187, v187\n\t" \
    "v_pk_mul_f32 v[184:185], s[50:51], v[184:185] op_sel_hi:[0,1]\n\t" \
    "v_pk_mul_f32 v[186:187], s[50:51], v[186:187] op_sel_hi:[0,1]\n\t" \
    "v_cvt_pk_bf16_f32 v188, v184, v185\n\t" \
    "v_cvt_pk_bf16_f32 v189, v186, v187\n\t" \
    "ds_write_b64 v79, v[188:189] offset:0\n\t" \
    "s_waitcnt vmcnt(2)\n\t" \
    "v_cvt_f32_i32 v192, v173\n\t" \
    "v_alignbit_b32 v193, v173, v172, 3\n\t" \
    "v_alignbit_b32 v195, v175, v174, 3\n\t" \
    "v_cvt_f32_i32_e64 v194, v175\n\t" \
    "v_cvt_f32_i32 v193, v193\n\t" \
    "v_cvt_f32_i32 v195, v195\n\t" \
    "v_pk_mul_f32 v[192:193], s[50:51], v[192:193] op_sel_hi:[0,1]\n\t" \
    "v_pk_mul_f32 v[194:195], s[50:51], v[194:195] op_sel_hi:[0,1]\n\t" \
    "v_cvt_pk_bf16_f32 v196, v192, v193\n\t" \
    "v_cvt_pk_bf16_f32 v197, v194, v195\n\t" \
    "ds_write_b64 v79, v[196:197] offset:64\n\t" \
    "s_waitcnt vmcnt(1)\n\t" \
    "v_cvt_f32_i32 v184, v177\n\t" \
    "v_alignbit_b32 v185, v177, v176, 3\n\t" \
    "v_alignbit_b32 v187, v179, v178, 3\n\t" \
    "v_cvt_f32_i32_e64 v186, v179\n\t" \
    "v_cvt_f32_i32 v185, v185\n\t" \
    "v_cvt_f32_i32 v187, v187\n\t" \
    "v_pk_mul_f32 v[184:185], s[50:51], v[184:185] op_sel_hi:[0,1]\n\t" \
    "v_pk_mul_f32 v[186:187], s[50:51], v[186:187] op_sel_hi:[0,1]\n\t" \
    "v_cvt_pk_bf16_f32 v188, v184, v185\n\t" \
    "v_cvt_pk_bf16_f32 v189, v186, v187\n\t" \
    "ds_write_b64 v79, v[188:189] offset:128\n\t" \
    "s_waitcnt vmcnt(0)\n\t" \
    "v_cvt_f32_i32 v192, v181\n\t" \
    "v_alignbit_b32 v193, v181, v180, 3\n\t" \
    "v_alignbit_b32 v195, v183, v182, 3\n\t" \
    "v_cvt_f32_i32_e64 v194, v183\n\t" \
    "v_cvt_f32_i32 v193, v193\n\t" \
    "v_cvt_f32_i32 v195, v195\n\t" \
    "v_pk_mul_f32 v[192:193], s[50:51], v[192:193] op_sel_hi:[0,1]\n\t" \
    "v_pk_mul_f32 v[194:195], s[50:51], v[194:195] op_sel_hi:[0,1]\n\t" \
    "v_cvt_pk_bf16_f32 v196, v192, v193\n\t" \
    "v_cvt_pk_bf16_f32 v197, v194, v195\n\t" \
    "ds_write_b64 v79, v[196:197] offset:192\n\t" \
    "v_bitop3_b32 v6, v168, v170, v172 bitop3:0x80\n\t" \
    "v_bitop3_b32 v6, v6, v174, v176 bitop3:0x80\n\t" \
    "v_bitop3_b32 v6, v6, v178, v180 bitop3:0x80\n\t" \
    "v_bitop3_b32 v6, v6, v182, 32 bitop3:0x80\n\t" \
    "v_cmp_eq_u32 vcc, 0, v6\n\t" \
    "s_cbranch_vccnz RETRY%=\n\t" \
    "ENTRY%=:\n\t" \
    "s_waitcnt lgkmcnt(0)\n\t" \
    "s_barrier\n\t" \
    "ds_read_b128 v[168:171], v73 offset:0\n\t" \
    "ds_read_b128 v[172:175], v73 offset:128\n\t" \
    "ds_read_b128 v[176:179], v73 offset:256\n\t" \
    "ds_read_b128 v[180:183], v73 offset:384\n\t" \
    "ds_read_b128 v[184:187], v73 offset:512\n\t" \
    "ds_read_b128 v[188:191], v73 offset:640\n\t" \
    "ds_read_b128 v[192:195], v73 offset:768\n\t" \
    "ds_read_b128 v[196:199], v73 offset:896\n\t" \
    "ds_read_b128 v[224:227], v83 offset:0\n\t" \
    "ds_read_b128 v[228:231], v83 offset:16\n\t" \
    "s_waitcnt lgkmcnt(9)\n\t" \
    "s_mov_b32 s48, s73\n\t" \
    "v_smfmac_f32_16x16x64_bf16 v[200:203], v[168:171], a[0:7], v72\n\t" \
    "ds_read_b128 v[104:107], v76 offset:0\n\t" \
    "v_smfmac_f32_16x16x64_bf16 v[204:207], v[168:171], a[128:135], v72\n\t" \
    "ds_read_b128 v[108:111], v76 offset:1024\n\t" \
    "v_smfmac_f32_16x16x64_bf16 v[208:211], v[168:171], a[64:71], v72\n\t" \
    "ds_read_b128 v[112:115], v76 offset:2048\n\t" \
    "v_smfmac_f32_16x16x64_bf16 v[212:215], v[168:171], a[192:199], v72\n\t" \
    "ds_read_b128 v[116:119], v76 offset:3072\n\t" \
    "s_waitcnt lgkmcnt(12)\n\t" \
    "s_add_u32 s43, s40, 2\n\t" \
    "v_smfmac_f32_16x16x64_bf16 v[200:203], v[172:175], a[8:15], v72\n\t" \
    "ds_read_b128 v[120:123], v76 offset:4096\n\t" \
    "v_smfmac_f32_16x16x64_bf16 v[204:207], v[172:175], a[136:143], v72\n\t" \
    "ds_read_b128 v[124:127], v76 offset:5120\n\t" \
    "v_smfmac_f32_16x16x64_bf16 v[208:211], v[172:175], a[72:79], v72\n\t" \
    "ds_read_b128 v[128:131], v76 offset:6144\n\t" \
    "v_smfmac_f32_16x16x64_bf16 v[212:215], v[172:175], a[200:207], v72\n\t" \
    "ds_read_b128 v[132:135], v76 offset:7168\n\t" \
    "s_waitcnt lgkmcnt(14)\n\t" \
    "s_add_u32 s49, s54, s85\n\t" \
    "v_smfmac_f32_16x16x64_bf16 v[200:203], v[176:179], a[16:23], v72\n\t" \
    "ds_read_b128 v[136:139], v76 offset:8192\n\t" \
    "v_smfmac_f32_16x16x64_bf16 v[204:207], v[176:179], a[144:151], v72\n\t" \
    "ds_read_b128 v[140:143], v76 offset:9216\n\t" \
    "v_smfmac_f32_16x16x64_bf16 v[208:211], v[176:179], a[80:87], v72\n\t" \
    "ds_read_b128 v[144:147], v76 offset:10240\n\t" \
    "v_smfmac_f32_16x16x64_bf16 v[212:215], v[176:179], a[208:215], v72\n\t" \
    "ds_read_b128 v[148:151], v76 offset:11264\n\t" \
    "s_waitcnt lgkmcnt(14)\n\t" \
    "s_mov_b32 s55, s54\n\t" \
    "v_smfmac_f32_16x16x64_bf16 v[200:203], v[180:183], a[24:31], v72\n\t" \
    "ds_read_b128 v[152:155], v76 offset:12288\n\t" \
    "v_smfmac_f32_16x16x64_bf16 v[204:207], v[180:183], a[152:159], v72\n\t" \
    "ds_read_b128 v[156:159], v76 offset:13312\n\t" \
    "v_smfmac_f32_16x16x64_bf16 v[208:211], v[180:183], a[88:95], v72\n\t" \
    "ds_read_b128 v[160:163], v76 offset:14336\n\t" \
    "v_smfmac_f32_16x16x64_bf16 v[212:215], v[180:183], a[216:223], v72\n\t" \
    "ds_read_b128 v[164:167], v76 offset:15360\n\t" \
    "s_waitcnt lgkmcnt(14)\n\t" \
    "s_min_u32 s43, s43, 63\n\t" \
    "v_smfmac_f32_16x16x64_bf16 v[200:203], v[184:187], a[32:39], v72\n\t" \
    "v_and_b32 v233, 0x7fff7fff, v224\n\t" \
    "v_smfmac_f32_16x16x64_bf16 v[204:207], v[184:187], a[160:167], v72\n\t" \
    "v_pk_max_u16 v87, v87, v233\n\t" \
    "v_smfmac_f32_16x16x64_bf16 v[208:211], v[184:187], a[96:103], v72\n\t" \
    "v_and_b32 v233, 0x7fff7fff, v225\n\t" \
    "v_smfmac_f32_16x16x64_bf16 v[212:215], v[184:187], a[224:231], v72\n\t" \
    "v_pk_max_u16 v87, v87, v233\n\t" \
    "v_smfmac_f32_16x16x64_bf16 v[200:203], v[188:191], a[40:47], v72\n\t" \
    "v_and_b32 v233, 0x7fff7fff, v226\n\t" \
    "v_smfmac_f32_16x16x64_bf16 v[204:207], v[188:191], a[168:175], v72\n\t" \
    "v_pk_max_u16 v87, v87, v233\n\t" \
    "v_smfmac_f32_16x16x64_bf16 v[208:211], v[188:191], a[104:111], v72\n\t" \
    "v_and_b32 v233, 0x7fff7fff, v227\n\t" \
    "v_smfmac_f32_16x16x64_bf16 v[212:215], v[188:191], a[232:239], v72\n\t" \
    "v_pk_max_u16 v87, v87, v233\n\t" \
    "v_smfmac_f32_16x16x64_bf16 v[200:203], v[192:195], a[48:55], v72\n\t" \
    "v_and_b32 v233, 0x7fff7fff, v228\n\t" \
    "v_smfmac_f32_16x16x64_bf16 v[204:207], v[192:195], a[176:183], v72\n\t" \
    "v_pk_max_u16 v87, v87, v233\n\t" \
    "v_smfmac_f32_16x16x64_bf16 v[208:211], v[192:195], a[112:119], v72\n\t" \
    "v_and_b32 v233, 0x7fff7fff, v229\n\t" \
    "v_smfmac_f32_16x16x64_bf16 v[212:215], v[192:195], a[240:247], v72\n\t" \
    "v_pk_max_u16 v87, v87, v233\n\t" \
    "v_smfmac_f32_16x16x64_bf16 v[200:203], v[196:199], a[56:63], v72\n\t" \
    "v_and_b32 v233, 0x7fff7fff, v230\n\t" \
    "v_smfmac_f32_16x16x64_bf16 v[204:207], v[196:199], a[184:191], v72\n\t" \
    "v_pk_max_u16 v87, v87, v233\n\t" \
    "v_smfmac_f32_16x16x64_bf16 v[208:211], v[196:199], a[120:127], v72\n\t" \
    "v_and_b32 v233, 0x7fff7fff, v231\n\t" \
    "v_smfmac_f32_16x16x64_bf16 v[212:215], v[196:199], a[248:255], v72\n\t" \
    "v_pk_max_u16 v87, v87, v233\n\t" \
    "s_waitcnt lgkmcnt(0)\n\t" \
    "s_cmp_eq_u32 s40, 63\n\t" \
    "v_smfmac_f32_16x16x64_bf16 v[220:223], v[168:171], v[104:111], v72\n\t" \
    "ds_write_b32 v84, v87\n\t" \
    "v_add_f32 v200, v200, v201\n\t" \
    "v_add_f32 v201, v202, v203\n\t" \
    "v_add_f32 v204, v204, v205\n\t" \
    "v_add_f32 v205, v206, v207\n\t" \
    "v_cvt_pk_bf16_f32 v202, v200, v201\n\t" \
    "v_cvt_pk_bf16_f32 v203, v204, v205\n\t" \
    "ds_write_b64 v74, v[202:203] offset:0\n\t" \
    "v_add_f32 v208, v208, v209\n\t" \
    "v_add_f32 v209, v210, v211\n\t" \
    "v_add_f32 v212, v212, v213\n\t" \
    "v_add_f32 v213, v214, v215\n\t" \
    "v_cvt_pk_bf16_f32 v210, v208, v209\n\t" \
    "v_cvt_pk_bf16_f32 v211, v212, v213\n\t" \
    "ds_write_b64 v74, v[210:211] offset:512\n\t" \
    "v_smfmac_f32_16x16x64_bf16 v[220:223], v[172:175], v[112:119], v72\n\t" \
    "v_smfmac_f32_16x16x64_bf16 v[220:223], v[176:179], v[120:127], v72\n\t" \
    "v_smfmac_f32_16x16x64_bf16 v[220:223], v[180:183], v[128:135], v72\n\t" \
    "v_smfmac_f32_16x16x64_bf16 v[220:223], v[184:187], v[136:143], v72\n\t" \
    "v_smfmac_f32_16x16x64_bf16 v[220:223], v[188:191], v[144:151], v72\n\t" \
    "s_waitcnt lgkmcnt(0)\n\t" \
    "s_barrier\n\t" \
    "ds_read_b128 v[132:135], v85 offset:0\n\t" \
    "ds_read_b64_tr_b16 v[224:225], v75 offset:0\n\t" \
    "ds_read_b64_tr_b16 v[226:227], v75 offset:512\n\t" \
    "ds_read_b128 v[200:203], v7 offset:0\n\t" \
    "ds_read_b128 v[240:243], v7 offset:4096\n\t" \
    "v_smfmac_f32_16x16x64_bf16 v[220:223], v[192:195], v[152:159], v72\n\t" \
    "ds_read_b64_tr_b16 v[228:229], v75 offset:1024\n\t" \
    "v_smfmac_f32_16x16x64_bf16 v[220:223], v[196:199], v[160:167], v72\n\t" \
    "ds_read_b64_tr_b16 v[230:231], v75 offset:1536\n\t" \
    "v_smfmac_f32_16x16x64_bf16 v[216:219], v[168:171], v[8:15], v72\n\t" \
    "ds_read_b128 v[204:207], v7 offset:1024\n\t" \
    "v_smfmac_f32_16x16x64_bf16 v[216:219], v[172:175], v[16:23], v72\n\t" \
    "ds_read_b128 v[244:247], v7 offset:5120\n\t" \
    "s_cbranch_scc1 LASTP%=\n\t" \
    "s_waitcnt lgkmcnt(4)\n\t" \
    "v_mfma_f32_16x16x32_bf16 v[104:107], v[224:227], v[224:227], 0\n\t" \
    "ds_read_b64_tr_b16 v[232:233], v75 offset:2048\n\t" \
    "ds_read_b64_tr_b16 v[234:235], v75 offset:2560\n\t" \
    "v_mfma_f32_16x16x32_bf16 v[108:111], v[200:203], v[224:227], 0\n\t" \
    "ds_read_b128 v[208:211], v7 offset:2048\n\t" \
    "ds_read_b128 v[248:251], v7 offset:6144\n\t" \
    "v_mfma_f32_16x16x32_bf16 v[112:115], v[240:243], v[224:227], 0\n\t" \
    "ds_read_b64_tr_b16 v[236:237], v75 offset:3072\n\t" \
    "ds_read_b64_tr_b16 v[238:239], v75 offset:3584\n\t" \
    "s_waitcnt lgkmcnt(6)\n\t" \
    "v_mfma_f32_16x16x32_bf16 v[104:107], v[228:231], v[228:231], v[104:107]\n\t" \
    "ds_read_b128 v[212:215], v7 offset:3072\n\t" \
    "ds_read_b128 v[252:255], v7 offset:7168\n\t" \
    "v_mfma_f32_16x16x32_bf16 v[108:111], v[204:207], v[228:231], v[108:111]\n\t" \
    "v_pk_max_u16 v132, v132, v133\n\t" \
    "v_pk_max_u16 v134, v134, v135\n\t" \
    "v_mfma_f32_16x16x32_bf16 v[112:115], v[244:247], v[228:231], v[112:115]\n\t" \
    "v_pk_max_u16 v132, v132, v134\n\t" \
    "v_pk_max_u16 v133, v132, s72\n\t" \
    "s_waitcnt lgkmcnt(4)\n\t" \
    "v_mfma_f32_16x16x32_bf16 v[104:107], v[232:235], v[232:235], v[104:107]\n\t" \
    "v_cmp_ne_u32 vcc, s72, v133\n\t" \
    "v_add_f32 v220, v220, v221\n\t" \
    "v_mfma_f32_16x16x32_bf16 v[108:111], v[208:211], v[232:235], v[108:111]\n\t" \
    "v_add_f32_e64 v222, v222, v223\n\t" \
    "v_cvt_pk_bf16_f32 v87, v220, v222\n\t" \
    "v_mfma_f32_16x16x32_bf16 v[112:115], v[248:251], v[232:235], v[112:115]\n\t" \
    "buffer_load_dwordx4 v[144:147], v80, s[60:63], s95 offen sc1\n\t" \
    "s_waitcnt lgkmcnt(0)\n\t" \
    "s_nop 0\n\t" \
    "v_mfma_f32_16x16x32_bf16 v[104:107], v[236:239], v[236:239], v[104:107]\n\t" \
    "buffer_load_dwordx4 v[148:151], v80, s[60:63], s95 offen offset:1024 sc1\n\t" \
    "v_mfma_f32_16x16x32_bf16 v[108:111], v[212:215], v[236:239], v[108:111]\n\t" \
    "buffer_load_dwordx4 v[116:119], v81, s[60:63], s92 offen sc1\n\t" \
    "v_mfma_f32_16x16x32_bf16 v[112:115], v[252:255], v[236:239], v[112:115]\n\t" \
    "buffer_load_dwordx4 v[120:123], v81, s[60:63], s93 offen sc1\n\t" \
    "s_cbranch_vccnz BIG%=\n\t" \
    "BIGR%=:\n\t" \
    "s_lshl_b32 s50, s48, 23\n\t" \
    "v_smfmac_f32_16x16x64_bf16 v[216:219], v[176:179], v[24:31], v72\n\t" \
    "s_sub_u32 s51, 0x8a000000, s50\n\t" \
    "buffer_load_dwordx4 v[124:127], v81, s[60:63], s94 offen sc1\n\t" \
    "v_smfmac_f32_16x16x64_bf16 v[216:219], v[180:183], v[32:39], v72\n\t" \
    "s_add_u32 s50, s50, 0xf3800000\n\t" \
    "v_smfmac_f32_16x16x64_bf16 v[216:219], v[184:187], v[40:47], v72\n\t" \
    "v_max3_f32 v142, v106, v107, v108\n\t" \
    "v_smfmac_f32_16x16x64_bf16 v[216:219], v[188:191], v[48:55], v72\n\t" \
    "v_max3_f32 v142, v142, v109, v110\n\t" \
    "v_max3_f32 v143, v111, v112, v113\n\t" \
    "v_max3_f32 v142, v142, v114, v115\n\t" \
    "v_max_f32 v142, v142, v143\n\t" \
    "v_mov_b32 v143, v142\n\t" \
    "v_smfmac_f32_16x16x64_bf16 v[216:219], v[192:195], v[56:63], v72\n\t" \
    "s_mov_b64 vcc, s[82:83]\n\t" \
    "v_permlane16_swap_b32 v142, v143\n\t" \
    "v_max_f32 v142, v142, v143\n\t" \
    "v_mov_b32 v143, v142\n\t" \
    "v_smfmac_f32_16x16x64_bf16 v[216:219], v[196:199], v[64:71], v72\n\t" \
    "v_mov_b32_e64 v196, s51\n\t" \
    "v_permlane32_swap_b32 v142, v143\n\t" \
    "v_max_f32 v142, v142, v143\n\t" \
    "v_mul_f32 v142, 0xbe0293ee, v142\n\t" \
    "v_pk_fma_f32 v[106:107], v[106:107], s[56:57], v[142:143] op_sel_hi:[1,0,0]\n\t" \
    "v_pk_fma_f32 v[108:109], v[108:109], s[56:57], v[142:143] op_sel_hi:[1,0,0]\n\t" \
    "v_pk_fma_f32 v[110:111], v[110:111], s[56:57], v[142:143] op_sel_hi:[1,0,0]\n\t" \
    "v_pk_fma_f32 v[112:113], v[112:113], s[56:57], v[142:143] op_sel_hi:[1,0,0]\n\t" \
    "v_pk_fma_f32 v[114:115], v[114:115], s[56:57], v[142:143] op_sel_hi:[1,0,0]\n\t" \
    "v_exp_f32 v106, v106\n\t" \
    "v_exp_f32 v107, v107\n\t" \
    "v_add_f32 v216, v216, v217\n\t" \
    "v_add_f32 v218, v218, v219\n\t" \
    "v_cvt_pk_bf16_f32 v95, v106, v107\n\t" \
    "v_cvt_pk_bf16_f32 v93, v216, v218\n\t" \
    "v_exp_f32 v108, v108\n\t" \
    "v_exp_f32 v109, v109\n\t" \
    "v_mfma_f32_16x16x16_bf16 v[176:179], v[88:89], v[94:95], 0\n\t" \
    "v_exp_f32_e64 v110, v110\n\t" \
    "v_mfma_f32_16x16x16_bf16 v[184:187], v[86:87], v[94:95], 0\n\t" \
    "v_exp_f32_e64 v111, v111\n\t" \
    "v_mfma_f32_16x16x16_bf16 v[180:183], v[92:93], v[94:95], 0\n\t" \
    "v_exp_f32 v112, v112\n\t" \
    "v_exp_f32 v113, v113\n\t" \
    "v_exp_f32 v114, v114\n\t" \
    "v_exp_f32 v115, v115\n\t" \
    "v_cvt_pk_bf16_f32 v168, v108, v109\n\t" \
    "v_cvt_pk_bf16_f32 v169, v110, v111\n\t" \
    "v_cvt_pk_bf16_f32 v170, v112, v113\n\t" \
    "v_cvt_pk_bf16_f32 v171, v114, v115\n\t" \
    "s_nop 1\n\t" \
    "v_mfma_f32_16x16x32_bf16 v[176:179], v[88:91], v[168:171], v[176:179]\n\t" \
    "s_nop 1\n\t" \
    "v_mfma_f32_16x16x32_bf16 v[180:183], v[96:99], v[168:171], v[180:183]\n\t" \
    "s_nop 1\n\t" \
    "v_mfma_f32_16x16x32_bf16 v[184:187], v[100:103], v[168:171], v[184:187]\n\t" \
    "s_nop 1\n\t" \
    "v_rcp_f32 v188, v176\n\t" \
    "s_nop 0\n\t" \
    "v_mul_f32 v188, v188, v196\n\t" \
    "s_nop 1\n\t" \
    "v_mov_b32_dpp v188, v188 quad_perm:[0,1,0,1] row_mask:0xf bank_mask:0xf\n\t" \
    "v_cndmask_b32_dpp v180, v184, v180, vcc quad_perm:[0,1,0,1] row_mask:0xf bank_mask:0xf\n\t" \
    "v_cndmask_b32_dpp v181, v185, v181, vcc quad_perm:[0,1,0,1] row_mask:0xf bank_mask:0xf\n\t" \
    "v_cndmask_b32_dpp v182, v186, v182, vcc quad_perm:[0,1,0,1] row_mask:0xf bank_mask:0xf\n\t" \
    "v_cndmask_b32_dpp v183, v187, v183, vcc quad_perm:[0,1,0,1] row_mask:0xf bank_mask:0xf\n\t" \
    "v_mul_f32_e64 v180, v188, v180\n\t" \
    "v_mul_f32 v181, v188, v181\n\t" \
    "v_mul_f32 v182, v188, v182\n\t" \
    "v_mul_f32 v183, v188, v183\n\t" \
    "v_cvt_rpi_i32_f32 v180, v180\n\t" \
    "v_cvt_rpi_i32_f32 v181, v181\n\t" \
    "v_cvt_rpi_i32_f32 v182, v182\n\t" \
    "v_cvt_rpi_i32_f32 v183, v183\n\t" \
    "v_ashrrev_i32 v191, 26, v181\n\t" \
    "v_lshl_or_b32 v190, v181, 6, 1\n\t" \
    "v_lshl_add_u32 v191, v180, 3, v191\n\t" \
    "buffer_atomic_add_x2 v[190:191], v77, s[60:63], s49 offen offset:0\n\t" \
    "v_ashrrev_i32_e64 v193, 26, v183\n\t" \
    "v_lshl_or_b32 v192, v183, 6, 1\n\t" \
    "v_lshl_add_u32 v193, v182, 3, v193\n\t" \
    "buffer_atomic_add_x2 v[192:193], v77, s[60:63], s49 offen offset:1024\n\t" \
    "s_memtime s[44:45]\n\t" \
    "SHADOW%=:\n\t" \
    "s_lshl_b32 s41, s40, 18\n\t" \
    "s_add_u32 s41, s41, s67\n\t" \
    "buffer_store_dword v93, v82, s[60:63], s41 offen sc1\n\t" \
    "buffer_store_dword v87, v82, s[60:63], s41 offen offset:1024 sc1\n\t" \
    "v_lshlrev_b32 v6, 3, v80\n\t" \
    "v_mov_b32 v142, s40\n\t" \
    "v_mov_b32 v143, s98\n\t" \
    "s_mov_b64 exec, s[78:79]\n\t" \
    "buffer_store_dwordx2 v[142:143], v6, s[60:63], s99 offen sc1\n\t" \
    "s_mov_b64 exec, -1\n\t" \
    "v_readfirstlane_b32 s41, v2\n\t" \
    "v_readfirstlane_b32 s42, v4\n\t" \
    "v_readfirstlane_b32 s47, v3\n\t" \
    "v_readfirstlane_b32 s48, v5\n\t" \
    "s_min_u32 s41, s41, s42\n\t" \
    "s_cmp_eq_u32 s47, s98\n\t" \
    "s_cselect_b32 s41, s41, 0\n\t" \
    "s_cmp_eq_u32 s48, s98\n\t" \
    "s_cselect_b32 s41, s41, 0\n\t" \
    "s_max_u32 s76, s76, s41\n\t" \
    "s_cmp_le_u32 s76, s43\n\t" \
    "s_cbranch_scc1 WAITP%=\n\t" \
    "PFETCH%=:\n\t" \
    "s_lshl_b32 s41, s43, 18\n\t" \
    "s_add_u32 s94, s41, s66\n\t" \
    "s_add_u32 s92, s41, s64\n\t" \
    "s_add_u32 s93, s92, 0x1000\n\t" \
    "s_lshl_b32 s41, s43, 20\n\t" \
    "s_add_u32 s95, s41, s65\n\t" \
    "s_xor_b32 s53, s53, 0x2000\n\t" \
    "s_waitcnt vmcnt(5)\n\t" \
    "v_add_u32 v6, s53, v81\n\t" \
    "ds_write_b128 v6, v[116:119]\n\t" \
    "ds_write_b128 v6, v[120:123] offset:4096\n\t" \
    "ds_write_b128 v6, v[124:127] offset:32768\n\t" \
    "v_mov_b64 v[96:97], v[144:145]\n\t" \
    "v_mov_b64 v[98:99], v[146:147]\n\t" \
    "v_mov_b64 v[100:101], v[148:149]\n\t" \
    "v_mov_b64 v[102:103], v[150:151]\n\t" \
    "s_cmp_eq_u32 s77, 0\n\t" \
    "s_cbranch_scc1 NOPJ%=\n\t" \
    "ds_read_b128 v[116:119], v7 offset:32768\n\t" \
    "ds_read_b128 v[120:123], v7 offset:33792\n\t" \
    "ds_read_b128 v[124:127], v7 offset:34816\n\t" \
    "ds_read_b128 v[128:131], v7 offset:35840\n\t" \
    "ds_read_b128 v[156:159], v85 offset:2048\n\t" \
    "ds_read_b128 v[160:163], v85 offset:3072\n\t" \
    "s_waitcnt lgkmcnt(0)\n\t" \
    "v_mfma_f32_16x16x32_bf16 v[104:107], v[224:227], v[116:119], 0\n\t" \
    "v_mfma_f32_16x16x32_bf16 v[108:111], v[200:203], v[116:119], v[156:159]\n\t" \
    "v_mfma_f32_16x16x32_bf16 v[112:115], v[240:243], v[116:119], v[160:163]\n\t" \
    "v_mfma_f32_16x16x32_bf16 v[104:107], v[228:231], v[120:123], v[104:107]\n\t" \
    "v_mfma_f32_16x16x32_bf16 v[108:111], v[204:207], v[120:123], v[108:111]\n\t" \
    "v_mfma_f32_16x16x32_bf16 v[112:115], v[244:247], v[120:123], v[112:115]\n\t" \
    "v_mfma_f32_16x16x32_bf16 v[104:107], v[232:235], v[124:127], v[104:107]\n\t" \
    "v_mfma_f32_16x16x32_bf16 v[108:111], v[208:211], v[124:127], v[108:111]\n\t" \
    "v_mfma_f32_16x16x32_bf16 v[112:115], v[248:251], v[124:127], v[112:115]\n\t" \
    "v_mfma_f32_16x16x32_bf16 v[104:107], v[236:239], v[128:131], v[104:107]\n\t" \
    "v_mfma_f32_16x16x32_bf16 v[108:111], v[212:215], v[128:131], v[108:111]\n\t" \
    "v_mfma_f32_16x16x32_bf16 v[112:115], v[252:255], v[128:131], v[112:115]\n\t" \
    "s_nop 7\n\t" \
    "v_cvt_pk_bf16_f32 v193, v106, v107\n\t" \
    "v_cvt_pk_bf16_f32 v194, v108, v109\n\t" \
    "v_cvt_pk_bf16_f32 v195, v110, v111\n\t" \
    "v_cvt_pk_bf16_f32 v196, v112, v113\n\t" \
    "v_cvt_pk_bf16_f32 v197, v114, v115\n\t" \
    "v_lshlrev_b32_e64 v106, 16, v193\n\t" \
    "v_and_b32 v107, 0xffff0000, v193\n\t" \
    "v_lshlrev_b32_e64 v108, 16, v194\n\t" \
    "v_and_b32 v109, 0xffff0000, v194\n\t" \
    "v_lshlrev_b32_e64 v110, 16, v195\n\t" \
    "v_and_b32 v111, 0xffff0000, v195\n\t" \
    "v_lshlrev_b32_e64 v112, 16, v196\n\t" \
    "v_and_b32 v113, 0xffff0000, v196\n\t" \
    "v_lshlrev_b32_e64 v114, 16, v197\n\t" \
    "v_and_b32 v115, 0xffff0000, v197\n\t" \
    "v_pk_mul_f32 v[106:107], v[106:107], s[58:59] op_sel_hi:[1,0]\n\t" \
    "v_pk_mul_f32 v[108:109], v[108:109], s[58:59] op_sel_hi:[1,0]\n\t" \
    "v_pk_mul_f32 v[110:111], v[110:111], s[58:59] op_sel_hi:[1,0]\n\t" \
    "v_pk_mul_f32 v[112:113], v[112:113], s[58:59] op_sel_hi:[1,0]\n\t" \
    "v_pk_mul_f32 v[114:115], v[114:115], s[58:59] op_sel_hi:[1,0]\n\t" \
    "v_cvt_pk_bf16_f32 v193, v106, v107\n\t" \
    "v_cvt_pk_bf16_f32 v194, v108, v109\n\t" \
    "v_cvt_pk_bf16_f32 v195, v110, v111\n\t" \
    "v_cvt_pk_bf16_f32 v196, v112, v113\n\t" \
    "v_cvt_pk_bf16_f32 v197, v114, v115\n\t" \
    "v_lshlrev_b32_e64 v106, 16, v193\n\t" \
    "v_and_b32 v107, 0xffff0000, v193\n\t" \
    "v_lshlrev_b32_e64 v108, 16, v194\n\t" \
    "v_and_b32 v109, 0xffff0000, v194\n\t" \
    "v_lshlrev_b32_e64 v110, 16, v195\n\t" \
    "v_and_b32 v111, 0xffff0000, v195\n\t" \
    "v_lshlrev_b32_e64 v112, 16, v196\n\t" \
    "v_and_b32 v113, 0xffff0000, v196\n\t" \
    "v_lshlrev_b32_e64 v114, 16, v197\n\t" \
    "v_and_b32 v115, 0xffff0000, v197\n\t" \
    "v_max3_f32 v142, v106, v107, v108\n\t" \
    "v_max3_f32 v143, v109, v110, v111\n\t" \
    "v_max3_f32 v142, v142, v112, v113\n\t" \
    "v_max3_f32 v143, v143, v114, v115\n\t" \
    "v_max_f32_e64 v142, v142, v143\n\t" \
    "v_mov_b32 v143, v142\n\t" \
    "s_nop 1\n\t" \
    "v_permlane16_swap_b32 v142, v143\n\t" \
    "v_max_f32 v142, v142, v143\n\t" \
    "v_mov_b32 v143, v142\n\t" \
    "s_nop 1\n\t" \
    "v_permlane32_swap_b32 v142, v143\n\t" \
    "v_max_f32 v142, v142, v143\n\t" \
    "v_mul_f32 v142, 0xbfb8aa3b, v142\n\t" \
    "v_pk_fma_f32 v[106:107], v[106:107], s[84:85], v[142:143] op_sel_hi:[1,0,0]\n\t" \
    "v_pk_fma_f32 v[108:109], v[108:109], s[84:85], v[142:143] op_sel_hi:[1,0,0]\n\t" \
    "v_pk_fma_f32 v[110:111], v[110:111], s[84:85], v[142:143] op_sel_hi:[1,0,0]\n\t" \
    "v_pk_fma_f32 v[112:113], v[112:113], s[84:85], v[142:143] op_sel_hi:[1,0,0]\n\t" \
    "v_pk_fma_f32 v[114:115], v[114:115], s[84:85], v[142:143] op_sel_hi:[1,0,0]\n\t" \
    "v_exp_f32 v106, v106\n\t" \
    "v_exp_f32 v107, v107\n\t" \
    "v_exp_f32 v108, v108\n\t" \
    "v_exp_f32 v109, v109\n\t" \
    "v_exp_f32 v110, v110\n\t" \
    "v_exp_f32 v111, v111\n\t" \
    "v_exp_f32 v112, v112\n\t" \
    "v_exp_f32 v113, v113\n\t" \
    "v_exp_f32 v114, v114\n\t" \
    "v_exp_f32 v115, v115\n\t" \
    "v_pk_add_f32 v[192:193], v[106:107], v[108:109]\n\t" \
    "v_pk_add_f32 v[194:195], v[110:111], v[112:113]\n\t" \
    "v_pk_add_f32 v[192:193], v[192:193], v[114:115]\n\t" \
    "v_pk_add_f32 v[192:193], v[192:193], v[194:195]\n\t" \
    "v_add_f32_e64 v142, v192, v193\n\t" \
    "v_mov_b32 v143, v142\n\t" \
    "s_nop 1\n\t" \
    "v_permlane16_swap_b32 v142, v143\n\t" \
    "v_add_f32 v142, v142, v143\n\t" \
    "v_mov_b32 v143, v142\n\t" \
    "s_nop 1\n\t" \
    "v_permlane32_swap_b32 v142, v143\n\t" \
    "v_add_f32 v142, v142, v143\n\t" \
    "v_rcp_f32 v142, v142\n\t" \
    "s_nop 0\n\t" \
    "v_pk_mul_f32 v[106:107], v[106:107], v[142:143] op_sel_hi:[1,0]\n\t" \
    "v_pk_mul_f32 v[108:109], v[108:109], v[142:143] op_sel_hi:[1,0]\n\t" \
    "v_pk_mul_f32 v[110:111], v[110:111], v[142:143] op_sel_hi:[1,0]\n\t" \
    "v_pk_mul_f32 v[112:113], v[112:113], v[142:143] op_sel_hi:[1,0]\n\t" \
    "v_pk_mul_f32 v[114:115], v[114:115], v[142:143] op_sel_hi:[1,0]\n\t" \
    "v_cvt_pk_bf16_f32 v192, v108, v109\n\t" \
    "v_cvt_pk_bf16_f32 v193, v110, v111\n\t" \
    "v_cvt_pk_bf16_f32 v194, v112, v113\n\t" \
    "v_cvt_pk_bf16_f32 v195, v114, v115\n\t" \
    "v_cvt_pk_bf16_f32 v196, v106, v107\n\t" \
    "s_lshl_b32 s41, s40, 16\n\t" \
    "s_add_u32 s41, s41, s69\n\t" \
    "s_lshl_b32 s42, s40, 14\n\t" \
    "s_add_u32 s42, s42, s70\n\t" \
    "buffer_store_dwordx4 v[192:195], v80, s[60:63], s41 offen sc1\n\t" \
    "buffer_store_dword v196, v82, s[60:63], s42 offen sc1\n\t" \
    "NOPJ%=:\n\t" \
    "s_cmp_eq_u32 s40, 63\n\t" \
    "s_cbranch_scc1 DONE%=\n\t" \
    "v_mov_b64_e64 v[200:201], 0\n\t" \
    "v_mov_b64 v[202:203], 0\n\t" \
    "v_mov_b64 v[204:205], 0\n\t" \
    "v_mov_b64 v[206:207], 0\n\t" \
    "v_mov_b64 v[208:209], 0\n\t" \
    "v_mov_b64 v[210:211], 0\n\t" \
    "v_mov_b64 v[212:213], 0\n\t" \
    "v_mov_b64 v[214:215], 0\n\t" \
    "v_mov_b64 v[216:217], 0\n\t" \
    "v_mov_b64 v[218:219], 0\n\t" \
    "v_mov_b64 v[220:221], 0\n\t" \
    "v_mov_b64 v[222:223], 0\n\t" \
    "v_mov_b32 v87, 0\n\t" \
    "s_xor_b32 s41, s54, 0x10000\n\t" \
    "s_add_u32 s41, s41, s71\n\t" \
    "s_mov_b32 s52, 0\n\t" \
    "s_mov_b64 exec, s[80:81]\n\t" \
    "buffer_store_dwordx4 v[216:219], v80, s[60:63], s41 offen sc1\n\t" \
    "s_add_u32 s41, s40, 3\n\t" \
    "s_cmp_le_i32 s76, s41\n\t" \
    "s_cselect_b64 exec, 1, 0\n\t" \
    "buffer_load_dwordx4 v[2:5], off, s[60:63], s75 sc1\n\t" \
    "s_mov_b64 exec, -1\n\t" \
    "s_add_u32 s40, s40, 1\n\t" \
    "s_add_u32 s54, s54, 0x8000\n\t" \
    "s_and_b32 s54, s54, 0x18000\n\t" \
    "v_add_u32_e64 v7, s53, v80\n\t" \
    "DLY%=:\n\t" \
    "s_memtime s[46:47]\n\t" \
    "s_waitcnt lgkmcnt(0)\n\t" \
    "s_sub_u32 s41, s46, s44\n\t" \
    "s_cmp_lt_u32 s41, s57\n\t" \
    "s_cbranch_scc1 DLY%=\n\t" \
    "s_mov_b32 s59, s46\n\t" \
    "s_add_u32 s41, s68, s55\n\t" \
    "s_add_u32 s42, s41, 0x1000\n\t" \
    "buffer_load_dwordx4 v[168:171], v78, s[60:63], s41 offen offset:0 sc1\n\t" \
    "buffer_load_dwordx4 v[172:175], v78, s[60:63], s41 offen offset:2048 sc1\n\t" \
    "buffer_load_dwordx4 v[176:179], v78, s[60:63], s42 offen offset:0 sc1\n\t" \
    "buffer_load_dwordx4 v[180:183], v78, s[60:63], s42 offen offset:2048 sc1\n\t" \
    "s_branch DEC%=\n\t" \
    "RETRY%=:\n\t" \
    "s_add_u32 s52, s52, 1\n\t" \
    "s_cmp_gt_u32 s52, 0x20000\n\t" \
    "s_cbranch_scc1 GIVEUP%=\n\t" \
    "s_add_u32 s41, s68, s55\n\t" \
    "s_add_u32 s42, s41, 0x1000\n\t" \
    "buffer_load_dwordx4 v[168:171], v78, s[60:63], s41 offen offset:0 sc1\n\t" \
    "buffer_load_dwordx4 v[172:175], v78, s[60:63], s41 offen offset:2048 sc1\n\t" \
    "buffer_load_dwordx4 v[176:179], v78, s[60:63], s42 offen offset:0 sc1\n\t" \
    "buffer_load_dwordx4 v[180:183], v78, s[60:63], s42 offen offset:2048 sc1\n\t" \
    "s_branch DEC%=\n\t" \
    "GIVEUP%=:\n\t" \
    "v_mov_b32_e64 v6, 2\n\t" \
    "s_movk_i32 s41, 0x400\n\t" \
    "buffer_store_dword v6, off, s[60:63], s41 sc1\n\t" \
    "s_branch ENTRY%=\n\t" \
    "WAITP%=:\n\t" \
    "s_mov_b32 s52, 0\n\t" \
    "WP1%=:\n\t" \
    "buffer_load_dwordx4 v[2:5], off, s[60:63], s75 sc1\n\t" \
    "s_waitcnt vmcnt(0)\n\t" \
    "v_readfirstlane_b32 s41, v2\n\t" \
    "v_readfirstlane_b32 s42, v4\n\t" \
    "v_readfirstlane_b32 s47, v3\n\t" \
    "v_readfirstlane_b32 s48, v5\n\t" \
    "s_min_u32 s41, s41, s42\n\t" \
    "s_cmp_eq_u32 s47, s98\n\t" \
    "s_cselect_b32 s41, s41, 0\n\t" \
    "s_cmp_eq_u32 s48, s98\n\t" \
    "s_cselect_b32 s41, s41, 0\n\t" \
    "s_mov_b32 s76, s41\n\t" \
    "s_cmp_gt_u32 s76, s43\n\t" \
    "s_cbranch_scc1 PFETCH%=\n\t" \
    "s_sleep 2\n\t" \
    "s_add_u32 s52, s52, 1\n\t" \
    "s_cmp_lt_u32 s52, 0x20000\n\t" \
    "s_cbranch_scc1 WP1%=\n\t" \
    "v_mov_b32 v6, 3\n\t" \
    "s_movk_i32 s41, 0x400\n\t" \
    "buffer_store_dword v6, off, s[60:63], s41 sc1\n\t" \
    "s_branch PFETCH%=\n\t" \
    "BIG%=:\n\t" \
    "v_lshrrev_b32 v133, 16, v132\n\t" \
    "v_and_b32 v132, 0xffff, v132\n\t" \
    "v_max_u32 v132, v132, v133\n\t" \
    "s_nop 1\n\t" \
    "v_max_u32_dpp v132, v132, v132 quad_perm:[1,0,3,2] row_mask:0xf bank_mask:0xf\n\t" \
    "s_nop 1\n\t" \
    "v_max_u32_dpp v132, v132, v132 quad_perm:[2,3,0,1] row_mask:0xf bank_mask:0xf\n\t" \
    "s_nop 1\n\t" \
    "v_max_u32_dpp v132, v132, v132 row_ror:4 row_mask:0xf bank_mask:0xf\n\t" \
    "s_nop 1\n\t" \
    "v_max_u32_dpp v132, v132, v132 row_ror:8 row_mask:0xf bank_mask:0xf\n\t" \
    "v_mov_b32 v133, v132\n\t" \
    "s_nop 1\n\t" \
    "v_permlane16_swap_b32 v132, v133\n\t" \
    "v_max_u32 v132, v132, v133\n\t" \
    "v_mov_b32 v133, v132\n\t" \
    "s_nop 1\n\t" \
    "v_permlane32_swap_b32 v132, v133\n\t" \
    "v_max_u32 v132, v132, v133\n\t" \
    "v_add_u32 v132, 1, v132\n\t" \
    "v_lshlrev_b32 v132, 16, v132\n\t" \
    "v_mul_f32 v132, s74, v132\n\t" \
    "v_lshrrev_b32 v132, 23, v132\n\t" \
    "v_max_u32 v132, s73, v132\n\t" \
    "v_min_u32 v132, 0xfe, v132\n\t" \
    "s_nop 0\n\t" \
    "v_readfirstlane_b32 s48, v132\n\t" \
    "s_nop 3\n\t" \
    "s_branch BIGR%=\n\t" \
    "LASTP%=:\n\t" \
    "ds_read_b64_tr_b16 v[232:233], v75 offset:2048\n\t" \
    "ds_read_b64_tr_b16 v[234:235], v75 offset:2560\n\t" \
    "ds_read_b128 v[208:211], v7 offset:2048\n\t" \
    "ds_read_b128 v[248:251], v7 offset:6144\n\t" \
    "ds_read_b64_tr_b16 v[236:237], v75 offset:3072\n\t" \
    "ds_read_b64_tr_b16 v[238:239], v75 offset:3584\n\t" \
    "ds_read_b128 v[212:215], v7 offset:3072\n\t" \
    "ds_read_b128 v[252:255], v7 offset:7168\n\t" \
    "v_smfmac_f32_16x16x64_bf16 v[216:219], v[176:179], v[24:31], v72\n\t" \
    "v_smfmac_f32_16x16x64_bf16 v[216:219], v[180:183], v[32:39], v72\n\t" \
    "v_smfmac_f32_16x16x64_bf16 v[216:219], v[184:187], v[40:47], v72\n\t" \
    "v_smfmac_f32_16x16x64_bf16 v[216:219], v[188:191], v[48:55], v72\n\t" \
    "v_smfmac_f32_16x16x64_bf16 v[216:219], v[192:195], v[56:63], v72\n\t" \
    "v_smfmac_f32_16x16x64_bf16 v[216:219], v[196:199], v[64:71], v72\n\t" \
    "s_nop 7\n\t" \
    "v_add_f32_e64 v216, v216, v217\n\t" \
    "v_add_f32 v218, v218, v219\n\t" \
    "v_add_f32 v220, v220, v221\n\t" \
    "v_add_f32 v222, v222, v223\n\t" \
    "v_cvt_pk_bf16_f32 v93, v216, v218\n\t" \
    "v_cvt_pk_bf16_f32 v87, v220, v222\n\t" \
    "s_waitcnt lgkmcnt(0)\n\t" \
    "s_waitcnt vmcnt(0)\n\t" \
    "s_mov_b32 s76, 64\n\t" \
    "s_branch SHADOW%=\n\t" \
    "DONE%=:\n\t"
#define CHAIN_CLOBBERS "v2", "v3", "s98", "s99", "v4", "v5", "v6", "v7", "v72", "v73", "v74", "v75", "v76", "v77", "v78", "v79", "v80", "v81", "v82", "v83", "v84", "v85", "v86", "v87", "v88", "v89", "v90", "v91", "v92", "v93", \
    "v94", "v95", "v96", "v97", "v98", "v99", "v100", "v101", "v102", "v103", "v104", "v105", "v106", "v107", "v108", "v109", "v110", "v111", "v112", "v113", "v114", "v115", "v116", \
    "v117", "v118", "v119", "v120", "v121", "v122", "v123", "v124", "v125", "v126", "v127", "v128", "v129", "v130", "v131", "v132", "v133", "v134", "v135", "v136", "v137", "v138", \
    "v139", "v140", "v141", "v142", "v143", "v144", "v145", "v146", "v147", "v148", "v149", "v150", "v151", "v152", "v153", "v154", "v155", "v156", "v157", "v158", "v159", "v160", \
    "v161", "v162", "v163", "v164", "v165", "v166", "v167", "v168", "v169", "v170", "v171", "v172", "v173", "v174", "v175", "v176", "v177", "v178", "v179", "v180", "v181", "v182", \
    "v183", "v184", "v185", "v186", "v187", "v188", "v189", "v190", "v191", "v192", "v193", "v194", "v195", "v196", "v197", "v198", "v199", "v200", "v201", "v202", "v203", "v204", \
    "v205", "v206", "v207", "v208", "v209", "v210", "v211", "v212", "v213", "v214", "v215", "v216", "v217", "v218", "v219", "v220", "v221", "v222", "v223", "v224", "v225", "v226", \
    "v227", "v228", "v229", "v230", "v231", "v232", "v233", "v234", "v235", "v236", "v237", "v238", "v239", "v240", "v241", "v242", "v243", "v244", "v245", "v246", "v247", "v248", \
    "v249", "v250", "v251", "v252", "v253", "v254", "v255", "s40", "s41", "s42", "s43", "s44", "s45", "s46", "s47", "s48", "s49", "s50", "s51", "s52", "s53", "s54", "s55", "s56", \
    "s57", "s58", "s59", "s60", "s61", "s62", "s63", "s64", "s65", "s66", "s67", "s68", "s69", "s70", "s71", "s72", "s73", "s74", "s75", "s76", "s77", "s78", "s79", "s80", "s81", \
    "s82", "s83", "s84", "s85", "s86", "s87", "s88", "s89", "s90", "s91", "s92", "s93", "s94", "s95", "s96", "s97", "vcc", "scc", "memory"

DI void chain_role(KP p, int h, int c, ldsp lds, const int wave, const u32 ep) {
    const int lane = lane_id(), tid = wave * 64 + lane;
    const int g = lane >> 4, li = lane & 15;
    const TrB trb = make_trb(lane);
    const __amdgpu_buffer_rsrc_t rws = __builtin_amdgcn_make_buffer_rsrc(p->ws, 0, (int)WS_END, 0x00020000);
    u32x32 wq[8];
    u32x32 b0[2];
    u32 m0v[8];
    {
        const rsrc_t rq = mk_rsrc(p->wq, 4u << 20), rk = mk_rsrc(p->wk, 4u << 20), rv = mk_rsrc(p->wv, 4u << 20), ro = mk_rsrc(p->wo, 4u << 20);
        const u32 lw = (u32)(size_t)lds + wave * 8192;
        CV cq;
#pragma unroll
        for (int rg = 0; rg < 4; ++rg) {
            const int r7 = (lane & 31) >> 2, rho = 32 * wave + 8 * rg + r7;
            cq.v[rg] = dperm((rho & 64) + posl(rho & 63)) * 8192 + (4 * (lane >> 5) + ((lane & 3) ^ ((2 * rg + (r7 >> 2)) & 3))) * 16;
        }
        const CV co = make_cv(lane, wave, 1024, false);
        __builtin_amdgcn_raw_buffer_store_b64((u32x2){0u, 0u}, rws, tid < 128 && lane < 16 ? lane * 8 : 0x40000000, (u32)OFF_ACC + wave * 32768 + (h * 4 + c) * 256, 16);
        asm volatile("" :: "s"(ep));
#pragma unroll
        for (int i = 0; i < 8; ++i) CHUNK_PIECE(ro, co, h * 131072 + c * 256, lw, L_IMG, i);
        u32x4 av[32];
        u32 vo[8];
#pragma unroll
        for (int t = 0; t < 8; ++t)
            vo[t] = dperm(64 * (t >> 2) + posl(32 * ((t & 3) >> 1) + 8 * (li >> 2) + 4 * (t & 1) + (li & 3))) * 8192 + g * 16;
        const u32 vs = wave * 1048576 + h * 256;
#pragma unroll
        for (int i = 0; i < 16; ++i) ALD4(av[i], rv, vo[i >> 2], vs, (i & 3) * 64, "");
        ALD4P(av[16], "a[160:163]", rv, vo[4], vs, 0);        ALD4P(av[17], "a[164:167]", rv, vo[4], vs, 64);
        ALD4P(av[18], "a[168:171]", rv, vo[4], vs, 128);        ALD4P(av[19], "a[172:175]", rv, vo[4], vs, 192);
        ALD4P(av[20], "a[176:179]", rv, vo[5], vs, 0);        ALD4P(av[21], "a[180:183]", rv, vo[5], vs, 64);
        ALD4P(av[22], "a[184:187]", rv, vo[5], vs, 128);        ALD4P(av[23], "a[188:191]", rv, vo[5], vs, 192);
        ALD4P(av[24], "a[224:227]", rv, vo[6], vs, 0);        ALD4P(av[25], "a[228:231]", rv, vo[6], vs, 64);
        ALD4P(av[26], "a[232:235]", rv, vo[6], vs, 128);        ALD4P(av[27], "a[236:239]", rv, vo[6], vs, 192);
        ALD4P(av[28], "a[240:243]", rv, vo[7], vs, 0);        ALD4P(av[29], "a[244:247]", rv, vo[7], vs, 64);
        ALD4P(av[30], "a[248:251]", rv, vo[7], vs, 128);        ALD4P(av[31], "a[252:255]", rv, vo[7], vs, 192);
        constexpr auto cbuf = [](int i) { return i == 2 || i == 3 ? (i - 2) * 32768 : 65536 + (i & 1) * 32768; };
        auto request = [&](auto ic) { sfor<8>([&](auto pc) { chunk_piece<cbuf(ic.value), pc.value>(ic.value < 4 ? rq : rk, cq, (ic.value & 3) * 1048576 + h * 256, lw); }); };
        sfor<4>(request);
        auto consume = [&](auto ic) {
            constexpr int m = ic.value >> 2, cc = ic.value & 3;
            if constexpr (ic.value >= 4) { wait_v<(ic.value < 7 ? 8 : 0)>(); BAR(); }
#pragma unroll
            for (int et = 0; et < 2; ++et)
#pragma unroll
                for (int k2 = 0; k2 < 2; ++k2) {
                    ldsp buf = lds + cbuf(ic.value) + 512 * wave + 16384 * k2;
                    const u32x2 t0 = trd(buf + trb.b[et][0]), t1 = trd(buf + trb.b[et][1]), t2 = trd(buf + trb.b[et][0] + 8192), t3 = trd(buf + trb.b[et][1] + 8192);
                    u32x32& w = wq[(2 * m + et) * 2 + (cc >> 1)];
                    const int o = ((cc & 1) * 2 + k2) * 8;
                    w[o] = t0.x; w[o + 1] = t0.y; w[o + 2] = t1.x; w[o + 3] = t1.y; w[o + 4] = t2.x; w[o + 5] = t2.y; w[o + 6] = t3.x; w[o + 7] = t3.y;
                }
            if constexpr (m == 0 && cc < 2) asm volatile("" : "+{a[0:31]}"(wq[0]), "+{a[64:95]}"(wq[2]));
            else if constexpr (m == 0) asm volatile("" : "+{a[32:63]}"(wq[1]), "+{a[96:127]}"(wq[3]));
            else if constexpr (cc < 2) asm volatile("" : "+{a[128:159]}"(wq[4]), "+{a[192:223]}"(wq[6]));
            else asm volatile("" : "+{a[160:191]}"(wq[5]), "+{a[224:255]}"(wq[7]));
            if constexpr (ic.value == 4 || ic.value == 5) { BAR(); request(std::integral_constant<int, ic.value + 2>{}); }
        };
        wait_v<0>(); PIN8(av, 0); PIN8(av, 8); PIN8A(av, 16); PIN8A(av, 24);
        BAR();
        sfor<4>(consume);
        BAR();
        request(std::integral_constant<int, 4>{}); request(std::integral_constant<int, 5>{});
        LOGT(2816 + blockIdx.x, 0);
        float ssv = 0, ss2 = 0;
        u32x4 wo[2][4];
        f32x4 ba[2][8];
        auto rd_wo = [&](int r) {
#pragma unroll
            for (int ks = 0; ks < 4; ++ks) wo[r & 1][ks] = wfrag(lds + L_IMG, trb, ks, 2 * (r & 3) + (r >> 2));
        };
        rd_wo(0);
        sfor<9>([&](auto cc) {
            constexpr int ct = cc.value, cp = ct - 1;
            if constexpr (ct + 1 < 8) rd_wo(ct + 1);
            if constexpr (ct == 7) {
#pragma unroll
                for (int i = 0; i < 8; ++i) m0v[i] = *(const GAS u32*)(p->mem0 + (tid >> 5) * DM + dperm((tid & 31) * 16 + 2 * i));
            }
            float sq0 = 0, sq1 = 0;
            u32x4 pk4;
            sfor<32>([&](auto ic) {
                constexpr int i = ic.value, t = i & 7, ks = i >> 3;
                if constexpr (ct < 8 && t < 4) { if constexpr (ks == 0) MF32Z(ba[ct & 1][t], av[4 * t], wo[ct & 1][0]); else MF32(ba[ct & 1][t], av[4 * t + ks], wo[ct & 1][ks]); }
                if constexpr (ct < 8 && t >= 4) { if constexpr (ks == 0) MFAZ(ba[ct & 1][t], av[4 * t], wo[ct & 1][0]); else MFA(ba[ct & 1][t], av[4 * t + ks], wo[ct & 1][ks]); }
                if constexpr (ct == 8 && i == 0) asm volatile("s_nop 7");
                if constexpr (cp >= 0) {
                    constexpr int tp = i >> 2, hf = (i >> 1) & 1;
                    if constexpr ((i & 3) == 0) asm volatile("" : "+v"(ba[cp & 1][tp]));
                    const float e0 = ba[cp & 1][tp][2 * hf], e1 = ba[cp & 1][tp][2 * hf + 1];
                    if constexpr ((i & 1) == 0) asm("v_fmac_f32 %0, %2, %2\n\tv_fmac_f32 %1, %3, %3" : "+v"(sq0), "+v"(sq1) : "v"(e0), "v"(e1));
                    else {
                        pk4[2 * (tp & 1) + hf] = pk(e0, e1);
                        if constexpr ((tp & 1) && hf) *(LAS u32x4*)(lds + L_BS + (((cp & 3) * 8 + 2 * wave + (tp >> 2)) * 2 + ((tp >> 1) & 1)) * 1024 + lane * 16) = pk4;
                    }
                }
                SB();
            });
            if constexpr (cp >= 0) {
                const float sm = xsum32(xsum16(sq0 + sq1));
                if ((cp & 3) == g) { if (cp < 4) ssv = sm; else ss2 = sm; }
            }
            if constexpr (ct == 4 || ct == 5) consume(std::integral_constant<int, ct>{});
            if constexpr (ct == 4) {
#pragma unroll
                for (int i = 0; i < 16; ++i) {
                    const u32x4 v = ldsr4(lds + L_BS + wave * 16384 + i * 1024 + lane * 16);
                    b0[i >> 3][4 * (i & 7)] = v.x; b0[i >> 3][4 * (i & 7) + 1] = v.y; b0[i >> 3][4 * (i & 7) + 2] = v.z; b0[i >> 3][4 * (i & 7) + 3] = v.w;
                }
                asm volatile("" : "+{v[8:39]}"(b0[0]), "+{v[40:71]}"(b0[1]));
                BAR();
            }
        });
        LOGT(2816 + blockIdx.x, 2);
        BAR();
        *(LAS float*)(lds + L_SS + (wave * 128 + lane) * 4) = ssv;
        *(LAS float*)(lds + L_SS + (wave * 128 + 64 + lane) * 4) = ss2;
        *(LAS u32x4*)(lds + L_IMG + (tid >> 5) * ROWB + (tid & 31) * 32) = (u32x4){m0v[0], m0v[1], m0v[2], m0v[3]};
        *(LAS u32x4*)(lds + L_IMG + (tid >> 5) * ROWB + (tid & 31) * 32 + 16) = (u32x4){m0v[4], m0v[5], m0v[6], m0v[7]};
        consume(std::integral_constant<int, 6>{});
        if (wave == 0) {
            const LAS float* sp = (const LAS float*)(lds + L_SS);
            const float a = (sp[lane] + sp[128 + lane]) + (sp[256 + lane] + sp[384 + lane]);
            const float b2 = (sp[64 + lane] + sp[192 + lane]) + (sp[320 + lane] + sp[448 + lane]);
            const float mx = wave_max(fmaxf(a, b2));
            if (lane == 0) put_cnt(p, CTL_BMX + (h * 4 + c) * 8, __builtin_bit_cast(u32, mx) | 1u, ep);
        }
        LOGT(2816 + blockIdx.x, 3);
        consume(std::integral_constant<int, 7>{});
    }
    LOGT(2560 + blockIdx.x, 0);
    LOGT(2560 + blockIdx.x, 1);
    const u32 l16 = lane * 16;
    {
        const int sl = (2 * (li >> 2) + (li & 1)) * 8 + ((li >> 1) & 1) * 4 + g;
        const u32 tab[14] = {li & 1 ? 0xEEEEu : 0x4444u,
                             (u32)(L_IMG + (li >> 1) * ROWB + 32 * g + 16 * (li & 1)),
                             (u32)(L_FI + (32 * wave + li) * 32 + 8 * (g ^ ((li >> 2) & 3))),
                             (u32)(L_FI + (4 * g + (li >> 2)) * 32 + 8 * ((li & 3) ^ g)),
                             (u32)(L_BS + wave * 16384) + l16,
                             (u32)((((c * 4 + wave) * 2) * 4 + (sl >> 4)) * 256 + (sl & 15) * 8),
                             (u32)((lane >> 3) * 256 + (lane & 7) * 16),
                             (u32)(L_IMG + (2 * ((lane >> 3) & 3) + ((lane >> 2) & 1)) * ROWB + 32 * (wave * 8 + (lane >> 5)) + 8 * (lane & 3)),
                             l16, (u32)tid * 16,
                             (u32)(((h & 3) * 16 + li) * 16 + g * 4),
                             (u32)(L_IMG + (2 * wave + (lane >> 5)) * ROWB + (lane & 31) * 32),
                             (u32)(L_SS + tid * 4), (u32)L_SS + l16};
        BAR();
#pragma unroll
        for (int k = 0; k < 14; ++k) *(LAS u32*)(lds + L_TAB + k * 1024 + tid * 4) = tab[k];
        if (wave == 1) {
            f32x4 cx0, cx1;
#pragma unroll
            for (int j = 0; j < 4; ++j) {
                cx0[j] = 4 * g + j <= c * 16 + li ? 0.f : -3.0e38f;
                cx1[j] = 16 + 4 * g + j <= c * 16 + li ? 0.f : -3.0e38f;
            }
            *(LAS f32x4*)(lds + L_SS + 2048 + l16) = cx0; *(LAS f32x4*)(lds + L_SS + 3072 + l16) = cx1;
        }
    }
    u32 fseen = 0;
    for (u32 spins = 0; fseen < 2; ++spins) {
        const int f = get_cnt(p, CTL_FQK + h * 128 + (lane & 1) * 8, ep);
        fseen = min(__builtin_amdgcn_readfirstlane(f), __builtin_amdgcn_readlane(f, 1));
        if (spins > SPIN_CAP) { __hip_atomic_store(ctlp(p, CTL_TMO), 3u, RLX_AGENT); break; }
        __builtin_amdgcn_s_sleep(1);
    }
    LOGT(2560 + blockIdx.x, 2);
    float hmax, bmax;
    {
        const u32x4 sa = __builtin_amdgcn_raw_buffer_load_b128(rws, l16, (u32)OFF_LNP, 16), ca = __builtin_amdgcn_raw_buffer_load_b128(rws, l16, (u32)OFF_LNP + 1024, 16);
        u32 v0, v1;
        for (u32 spins = 0;; ++spins) {
            v0 = get_cnt(p, CTL_BMX + lane * 8, ep); v1 = get_cnt(p, CTL_BMX + 512 + lane * 8, ep);
            if (__all(v0 & v1 & 1u)) break;
            if (spins > SPIN_CAP) { __hip_atomic_store(ctlp(p, CTL_TMO), 1u, RLX_AGENT); break; }
            __builtin_amdgcn_s_sleep(1);
        }
        float gm = 0, sq = 0;
#pragma unroll
        for (int j = 0; j < 4; ++j) { gm = fmaxf(gm, fmaxf(fabsf(lo_f(ca[j])), fabsf(hi_f(ca[j])))); sq += lo_f(sa[j]) * lo_f(sa[j]) + hi_f(sa[j]) * hi_f(sa[j]); }
        hmax = (22.7f * wave_max(gm) + sqrtf(wave_sum(sq))) * (1.f / 22.7f);
        bmax = 1.1f * 22.7f * sqrtf(wave_max(fmaxf(__builtin_bit_cast(float, v0 + 0u), __builtin_bit_cast(float, v1 + 0u))));
    }
    const u32 thrm = __builtin_amdgcn_readfirstlane((max(__builtin_bit_cast(u32, hmax) >> 16, 1u) - 1u) * 0x10001u);
    const u32 bmu = __builtin_amdgcn_readfirstlane(__builtin_bit_cast(u32, bmax));
    const u32 eb0 = __builtin_amdgcn_readfirstlane(min(max(__builtin_bit_cast(u32, bmax * hmax) >> 23, 26u), 254u));
    LOGT(2560 + blockIdx.x, 3);
    asm volatile(CHAIN_ASM
        :
        : "{a[0:31]}"(wq[0]), "{a[32:63]}"(wq[1]), "{a[64:95]}"(wq[2]), "{a[96:127]}"(wq[3]), "{a[128:159]}"(wq[4]), "{a[160:191]}"(wq[5]), "{a[192:223]}"(wq[6]), "{a[224:255]}"(wq[7]),
          "{v[8:39]}"(b0[0]), "{v[40:71]}"(b0[1]), [tab] "v"((u32)L_TAB + tid * 4), [wsp] "s"(p->ws), [wsn] "s"((u32)WS_END),
          [kx] "s"((u32)OFF_KF + h * 8192), [ux] "s"((u32)OFF_UF + (h * 32 + c * 8 + wave * 2) * 1024), [qx] "s"((u32)OFF_QF + h * 8192 + (c & 1) * 4096),
          [zs] "s"((u32)OFF_ZF + ((h >> 2) * 32 + c * 8 + wave * 2) * 1024), [gat] "s"((u32)OFF_ACC + wave * 8192), [pxf] "s"((u32)OFF_PXF + (h * 2 + c) * 1024),
          [pmf] "s"((u32)OFF_PMF + ((h >> 2) * 2 + c) * 1024), [zero] "s"((u32)OFF_ACC + (h * 4 + c) * 256), [thrm] "s"(thrm), [eb0] "s"(eb0), [bmax] "s"(bmu),
          [fqk] "s"(CTL_FQK + h * 128), [fseen] "s"(fseen), [ep] "s"(ep), [wid] "s"((h * 4 + c) * 4 + wave)
        : CHAIN_CLOBBERS);
    DRAIN_BAR();
    if (wave == 0 && lane_id() == 0) put_tag(p, CTL_FIN + (h * 4 + c) * 4, ep);
}

DI u32x2 resid(const u32x2& xv, const f32x4& gv, const f32x4& a) {
    const u32 a0 = pk(a[0], a[1]), a1 = pk(a[2], a[3]);
    const u32 b0 = pk(gv[0] * lo_f(a0), gv[1] * hi_f(a0)), b1 = pk(gv[2] * lo_f(a1), gv[3] * hi_f(a1));
    return (u32x2){pk(lo_f(xv.x) + lo_f(b0), hi_f(xv.x) + hi_f(b0)), pk(lo_f(xv.y) + lo_f(b1), hi_f(xv.y) + hi_f(b1))};
}
template <int NCT>
DI void job_py(KP p, int seg, int cq, ldsp lds, int wave, const u32 ep) {
    FRESH(p); FRESH(wave);
    const int lane = lane_id();
    const int g = lane >> 4, li = lane & 15;
    constexpr int RD = NCT == 8 ? 5 : 10, RU = RD < 8 ? RD : 8;
    const rsrc_t rws = mk_rsrc(p->ws, (u32)WS_END);
    const u32 l16 = lane * 16;
    f32x4 acc[NCT][2];
#pragma unroll
    for (int i = 0; i < NCT; ++i) { acc[i][0] = (f32x4){0, 0, 0, 0}; acc[i][1] = (f32x4){0, 0, 0, 0}; }
    u32x4 ru[RD][NCT], rp[RD][2];
    auto fetch_u = [&](int hh) {
        const u32 ub = hh < 8 ? (u32)OFF_UF + ((seg * NH + wave * 8 + hh) * 32 + cq * NCT) * 1024 : (u32)OFF_ZF + ((seg * 8 + wave * 2 + hh - 8) * 32 + cq * NCT) * 1024;
#pragma unroll
        for (int i = 0; i < NCT; ++i) ru[hh % RD][i] = __builtin_amdgcn_raw_buffer_load_b128(rws, l16 + (i & 3) * 1024, ub + (i >> 2) * 4096, LD_AUX);
    };
    auto fetch_p = [&](int hh) {
        const u32 pb = hh < 8 ? (u32)OFF_PXF + (seg * NH + wave * 8 + hh) * 2048 : (u32)OFF_PMF + (seg * 8 + wave * 2 + hh - 8) * 2048;
#pragma unroll
        for (int i = 0; i < 2; ++i) rp[hh % RD][i] = __builtin_amdgcn_raw_buffer_load_b128(rws, l16 + i * 1024, pb, LD_AUX);
    };
    wg_wait(p, wave, 4u, [&]() { return seg + 1 - get_cnt(p, CTL_FQK + (lane & 31) * 128 + 8, ep); });
#pragma unroll
    for (int hh = 0; hh < RU; ++hh) fetch_u(hh);
    constexpr int NQ = NCT / 4;
    const int ct0 = NCT == 8 ? 2 * wave : 2 * (wave >> 1), qt0 = NCT == 8 ? 0 : (wave & 1);
    u32x2 xv[NQ][2]; f32x4 gv[2];
#pragma unroll
    for (int i = 0; i < 2 * NQ; ++i) xv[i >> 1][i & 1] = *(const GAS u32x2*)(p->x + (size_t)(seg * 32 + (qt0 + (i >> 1)) * 16 + li) * DM + (cq * NCT + ct0 + (i & 1)) * 16 + 4 * g);
#pragma unroll
    for (int i = 0; i < 2; ++i) gv[i] = __builtin_bit_cast(f32x4, __builtin_amdgcn_raw_buffer_load_b128(rws, g * 16, (u32)OFF_MOD + (2 * DM + (cq * NCT + ct0 + i) * 16) * 4, LD_AUX));
    const u32x4 lg = __builtin_amdgcn_raw_buffer_load_b128(rws, g * 16, (u32)OFF_LNP + 4096 + (cq * NCT + ct0) * 32, LD_AUX), ls = __builtin_amdgcn_raw_buffer_load_b128(rws, g * 16, (u32)OFF_LNP + 3072 + (cq * NCT + ct0) * 32, LD_AUX);
    SB();
    wg_wait(p, wave, 5u, [&]() { const int pr = get_cnt(p, CTL_PROG + (blockIdx.x & 7) * 128, ep); return seg < NSEG - 2 || pr < NSEG - 1 ? seg + 2 - pr : (no_tag(p, CTL_FIN + lane * 4, ep) | no_tag(p, CTL_FIN + 256 + lane * 4, ep)); });
    JLOG(0);
#pragma unroll
    for (int hh = 0; hh < RD; ++hh) { fetch_p(hh); if (hh >= RU) fetch_u(hh); }
#pragma unroll
    for (int hh = 0; hh < 10; ++hh) {
        SB();
#pragma unroll
        for (int ct = 0; ct < NCT; ++ct) { acc[ct][0] = mfma32(as_bf(ru[hh % RD][ct]), as_bf(rp[hh % RD][0]), acc[ct][0]); acc[ct][1] = mfma32(as_bf(ru[hh % RD][ct]), as_bf(rp[hh % RD][1]), acc[ct][1]); }
        SB();
        if (hh + RD < 10) { fetch_p(hh + RD); fetch_u(hh + RD); }
    }
    SB(); JLOG(1);
#pragma unroll
    for (int ct = 0; ct < NCT; ++ct)
#pragma unroll
        for (int qt = 0; qt < 2; ++qt) *(LAS f32x4*)(lds + ((wave * NCT + ct) * 2 + qt) * 1024 + l16) = acc[ct][qt];
    BAR();
    u32x4 x2[NQ];
    const int part = (cq * NCT + ct0) >> 1;
#pragma unroll
    for (int q = 0; q < NQ; ++q) {
        u32x2 r[2];
#pragma unroll
        for (int i = 0; i < 2; ++i) {
            f32x4 a = *(LAS f32x4*)(lds + ((ct0 + i) * 2 + qt0 + q) * 1024 + l16);
#pragma unroll
            for (int w = 1; w < 4; ++w) a += *(LAS f32x4*)(lds + ((w * NCT + ct0 + i) * 2 + qt0 + q) * 1024 + l16);
            r[i] = resid(xv[q][i], gv[i], a);
        }
        x2[q] = cat(r[0], r[1]);
        float sm = 0, sq = 0;
#pragma unroll
        for (int j = 0; j < 4; ++j) sm += lo_f(x2[q][j]) + hi_f(x2[q][j]);
        const float mp = xsum32(xsum16(sm)) * (1.f / 32);
#pragma unroll
        for (int j = 0; j < 4; ++j) { const float d0 = lo_f(x2[q][j]) - mp, d1 = hi_f(x2[q][j]) - mp; sq += d0 * d0 + d1 * d1; }
        sq = xsum32(xsum16(sq));
        if (g == 0) __builtin_amdgcn_raw_buffer_store_b64((u32x2){__builtin_bit_cast(u32, mp), __builtin_bit_cast(u32, sq) | 0x80000000u}, rws, li * 128 + part * 8,
                                                          (u32)OFF_STAT + seg * 4096 + (qt0 + q) * 2048, ST_AUX);
    }
#pragma unroll
    for (int q = 0; q < NQ; ++q) st16(x2[q], rws, l16, (u32)OFF_X2 + ((seg * 2 + qt0 + q) * 16 + part) * 1024);
    JLOG(2);
#pragma unroll
    for (int q = 0; q < NQ; ++q) {
        u32x4 a, b;
        for (u32 spins = 0;; ++spins) {
            asm volatile("" ::: "memory");
            a = __builtin_amdgcn_raw_buffer_load_b128(rws, li * 128 + g * 32, (u32)OFF_STAT + seg * 4096 + (qt0 + q) * 2048, LD_AUX);
            b = __builtin_amdgcn_raw_buffer_load_b128(rws, li * 128 + g * 32 + 16, (u32)OFF_STAT + seg * 4096 + (qt0 + q) * 2048, LD_AUX);
            if (__all((a.y & a.w & b.y & b.w) >> 31)) break;
            if (spins > SPIN_CAP) { __hip_atomic_store(ctlp(p, CTL_TMO), 13u, RLX_AGENT); break; }
            __builtin_amdgcn_s_sleep(2);
        }
        const float m0 = __builtin_bit_cast(float, a.x + 0u), m1 = __builtin_bit_cast(float, a.z + 0u), m2 = __builtin_bit_cast(float, b.x + 0u), m3 = __builtin_bit_cast(float, b.z + 0u);
        const float mean = xsum32(xsum16((m0 + m1) + (m2 + m3))) * (1.f / 16);
        const float dev = (m0 - mean) * (m0 - mean) + (m1 - mean) * (m1 - mean) + (m2 - mean) * (m2 - mean) + (m3 - mean) * (m3 - mean);
        const float m2s = -((__builtin_bit_cast(float, a.y + 0u) + __builtin_bit_cast(float, a.w + 0u)) + (__builtin_bit_cast(float, b.y + 0u) + __builtin_bit_cast(float, b.w + 0u)));
        const float var = xsum32(xsum16(m2s + 32.f * dev)) * (1.f / DM);
        const float mb = rbf(mean), rs = __builtin_amdgcn_rcpf(rbf(__builtin_amdgcn_sqrtf(rbf(rbf(var) + 1e-5f))));
        u32x4 hh;
#pragma unroll
        for (int j = 0; j < 4; ++j) {
            u32 t = pk(lo_f(x2[q][j]) - mb, hi_f(x2[q][j]) - mb);
            t = pk(lo_f(t) * rs, hi_f(t) * rs);
            t = pk(lo_f(t) * lo_f(lg[j]), hi_f(t) * hi_f(lg[j]));
            hh[j] = pk(lo_f(t) + lo_f(ls[j]), hi_f(t) + hi_f(ls[j]));
        }
        st16(hh, rws, l16, (u32)OFF_H2F + ((seg * 2 + qt0 + q) * 16 + part) * 1024);
    }
    JLOG(3);
    DRAIN_BAR();
    JLOG(4);
    if (wave == 0 && lane < NQ) put_tag(p, CTL_H2 + seg * 128 + (cq * NQ + lane) * 4, ep);
}

DI float swiglu(float g, float u) { const float gv = rbf(g); return rbf(gv * __builtin_amdgcn_rcpf(1.f + __builtin_amdgcn_exp2f(-LOG2E * gv))) * rbf(u); }
DI u32x4 swiglu8(const f32x4& g0, const f32x4& g1, const f32x4& u0, const f32x4& u1) {
    f32x4 t0, t1;
#pragma unroll
    for (int j = 0; j < 4; ++j) { t0[j] = swiglu(g0[j], u0[j]); t1[j] = swiglu(g1[j], u1[j]); }
    return pack2(t0, t1);
}
DI void role_ffn1(KP p, int set, int nc, ldsp lds, int wave, const u32 ep) {
    FRESH(p); FRESH(wave);
    const int lane = lane_id();
    const TrB trb = make_trb(lane);
    const rsrc_t rws = mk_rsrc(p->ws, (u32)WS_END);
    const u32 lw = (u32)(size_t)lds + wave * 8192, l16 = lane * 16;
    u32x4 w[64];
    {
        const CV cv = make_cv(lane, wave, FF * 2, true);
        auto ga = [&](ldsp b, int i) { return wfrag(b + 512 * wave, trb, i >> 1, i & 1); };
        load_w(w, mk_rsrc(p->wg, DM * FF * 2), cv, nc * 256, 128 * FF * 2, mk_rsrc(p->wu, DM * FF * 2), cv, nc * 256, 128 * FF * 2, lds, lw, ga, ga, []() {}, []() {}, []() {});
    }
    for (int s = set; s < NSEG; s += s < 56 ? 2 : 4) {
        LOGT(s * 32 + nc, 0);
        wg_wait(p, wave, 6u, [&]() { return no_tag(p, CTL_H2 + s * 128 + (lane & 7) * 4, ep); });
        LOGT(s * 32 + nc, 1);
        u32x4 hx[8];
#pragma unroll
        for (int i = 0; i < 8; ++i) hx[i] = __builtin_amdgcn_raw_buffer_load_b128(rws, l16 + (i & 3) * 1024, (u32)OFF_H2F + s * 32768 + wave * 8192 + (i >> 2) * 4096, LD_AUX);
#pragma unroll
        for (int i = 0; i < 8; ++i) *(LAS u32x4*)(lds + wave * 8192 + i * 1024 + l16) = hx[i];
        BAR();
        f32x4 acc[2][4];
        seg_gemm(acc, w, lds + l16, [](auto) {});
#pragma unroll
        for (int tt = 0; tt < 2; ++tt)
            st16(swiglu8(acc[tt][0], acc[tt][1], acc[tt][2], acc[tt][3]), rws, l16, (u32)OFF_TF + ((s * 2 + tt) * 56 + nc * 4 + wave) * 1024);
        DRAIN_BAR();
        if (wave == 0 && lane == 0) put_tag(p, CTL_CT + s * 128 + nc * 4, ep);
        LOGT(s * 32 + nc, 2);
    }
}

DI void role_ffn2(KP p, int set, int cc, ldsp lds, int wave, const u32 ep) {
    FRESH(p); FRESH(wave);
    const int lane = lane_id();
    const int g = lane >> 4, li = lane & 15;
    const TrB trb = make_trb(lane);
    const rsrc_t rws = mk_rsrc(p->ws, (u32)WS_END), rd = mk_rsrc(p->wd, DM * FF * 2), ro = mk_rsrc(p->out, NSEG * 32 * DM * 2);
    const u32 lw = (u32)(size_t)lds + wave * 8192, l16 = lane * 16;
    u32x4 w[56];
    {
        const CV cv = make_cv(lane, wave, 1024, true);
        sfor<2>([&](auto rc) {
            constexpr int nb = rc.value == 0 ? 4 : 3;
            sfor<nb * 8>([&](auto ic) {
                constexpr int b = ic.value >> 3, i = ic.value & 7;
                const u32 so = (rc.value * 8 + b * 2 + (i & 1)) * 131072 + cc * 128, vo = cv.v[i >> 1], la = lw;
                const rsrc_t r = rd;
                DMA16(r, vo, so, la, b * 32768 + i * 1024, 0);
            });
            wait_v<0>(); BAR();
#pragma unroll
            for (int i = 0; i < nb * 8; ++i) w[rc.value * 32 + i] = wfrag(lds + (i >> 3) * 32768 + wave * 8192, trb, 0, i & 7);
            BAR();
        });
    }
    const int col = cc * 64 + (wave >> 1) * 32 + 4 * g, tq = wave & 1;
    const f32x4 gv0 = __builtin_bit_cast(f32x4, __builtin_amdgcn_raw_buffer_load_b128(rws, col * 4, (u32)OFF_MOD + 5 * DM * 4, LD_AUX)),
                gv1 = __builtin_bit_cast(f32x4, __builtin_amdgcn_raw_buffer_load_b128(rws, col * 4 + 64, (u32)OFF_MOD + 5 * DM * 4, LD_AUX));
    for (int s = set; s < NSEG; s += s < 56 ? 2 : 4) {
        LOGT(s * 32 + 16 + cc, 0);
        wg_wait(p, wave, 9u, [&]() { return no_tag(p, CTL_CT + s * 128 + min(lane, 13) * 4, ep); });
        LOGT(s * 32 + 16 + cc, 1);
        u32x4 tf[28];
#pragma unroll
        for (int i = 0; i < 28; ++i) tf[i] = __builtin_amdgcn_raw_buffer_load_b128(rws, l16, (u32)OFF_TF + ((s * 2 + (i & 1)) * 56 + (i >> 1) * 4 + wave) * 1024, LD_AUX);
        const u32x4 xv = __builtin_amdgcn_raw_buffer_load_b128(rws, l16, (u32)OFF_X2 + ((s * 2 + tq) * 16 + cc * 2 + (wave >> 1)) * 1024, LD_AUX);
        SB();
        f32x4 acc[4][2];
#pragma unroll
        for (int i = 0; i < 8; ++i) acc[i >> 1][i & 1] = (f32x4){0, 0, 0, 0};
#pragma unroll
        for (int kc = 0; kc < 14; ++kc) {
#pragma unroll
            for (int i = 0; i < 8; ++i) acc[i >> 1][i & 1] = mfma32(as_bf(w[4 * kc + (i >> 1)]), as_bf(tf[2 * kc + (i & 1)]), acc[i >> 1][i & 1]);
            SB();
        }
#pragma unroll
        for (int i = 0; i < 8; ++i) *(LAS f32x4*)(lds + (wave * 8 + i) * 1024 + l16) = acc[i >> 1][i & 1];
        BAR();
#pragma unroll
        for (int i = 0; i < 2; ++i) {
            const int t = ((wave >> 1) * 2 + i) * 2 + tq;
            f32x4 a = *(LAS f32x4*)(lds + t * 1024 + l16);
#pragma unroll
            for (int ww = 1; ww < 4; ++ww) a += *(LAS f32x4*)(lds + (ww * 8 + t) * 1024 + l16);
            *(LAS u32x2*)(lds + 32768 + (tq * 16 + li) * 144 + (wave >> 1) * 64 + i * 32 + g * 8) = resid(i ? (u32x2){xv.z, xv.w} : (u32x2){xv.x, xv.y}, i ? gv1 : gv0, a);
        }
        BAR();
        st16(ldsr4(lds + 32768 + (wave * 8 + (lane >> 3)) * 144 + (lane & 7) * 16), ro, (wave * 8 + (lane >> 3)) * 1024 + (lane & 7) * 16, s * 32768 + cc * 128);
        if (s == NSEG - 1 && cc == 0 && wave == 0 && lane == 0) put_tag(p, CTL_EP, ep);
        LOGT(s * 32 + 16 + cc, 2);
    }
}

__global__ void __launch_bounds__(256, 1) k_main(Params) {
    KP p = (KP)__builtin_amdgcn_kernarg_segment_ptr();
    extern __shared__ __attribute__((aligned(16))) unsigned char smem[];
    ldsp lds = (ldsp)smem;
    const int x = blockIdx.x & 7, y = blockIdx.x >> 3, wave = __builtin_amdgcn_readfirstlane(threadIdx.x >> 6);
    const u32 ep = __builtin_amdgcn_readfirstlane(ld_dev(ctlp(p, CTL_EP))) + 1u;
    LOGT(2048 + blockIdx.x, 0);
    if (x < 4) chain_role(p, x * 8 + (y >> 2), y & 3, lds, wave, ep);
    else {
        int role = 0, first = 0, slice = 0;
        if (y < 8) {
            role_proj<0>(p, (x - 4) * 8 + y, lds, wave, ep);
            LOGT(2048 + blockIdx.x, 1);
            role = 3; first = PY_SPLIT + x - 4; slice = y;
        } else if (y < 16) {
            const int id = (x - 4) * 8 + y - 8;
            role_proj<1>(p, id, lds, wave, ep);
            LOGT(2048 + blockIdx.x, 1);
            if (id < 14) { role = 1; first = 58; slice = id; } else if (id < 30) { role = 2; first = 58 + ((id - 14) >> 3); slice = (id - 14) & 7; } else { role = 3; first = NSEG - 1; slice = id - 24; }
        } else {
            const int id = (y - 16) * 4 + x - 4;
            for (int k = 0; k < 2; ++k) {
                if (id < 48 && (id < 16) == (k == 0)) job_mod(p, id, lds, wave, ep);
                if (k == 0) { LOGT(2560 + blockIdx.x, 0); job_ln1(p, (id + 48) & 63, lds, wave, ep); LOGT(2560 + blockIdx.x, 3); }
            }
            wg_wait(p, wave, 14u, [&]() { return no_tag(p, CTL_MODF + min(lane_id(), 47) * 4, ep); });
            if (id < 28) { role = 1; first = id >= 14; slice = id >= 14 ? id - 14 : id; }
            else if (id < 44) { role = 2; first = (id - 28) >> 3; slice = (id - 28) & 7; }
            else {
                for (int s = (id - 44) >> 2; s < PY_SPLIT; s += 5) { LOGT(s * 32 + 24 + (id & 3), 0); JSET(s * 32 + 24 + (id & 3)); job_py<8>(p, s, id & 3, lds, wave, ep); LOGT(s * 32 + 24 + (id & 3), 2); }
                if (id < 58) { role = 1; first = 59; slice = id - 44; } else { role = 3; first = NSEG - 1; slice = id - 58; }
            }
        }
        if (role == 1) role_ffn1(p, first, slice, lds, wave, ep);
        else if (role == 2) role_ffn2(p, first, slice, lds, wave, ep);
        else for (int s = first; s < NSEG - (first < NSEG - 1); s += 4) { LOGT(s * 32 + 24 + slice, 0); JSET(s * 32 + 24 + slice); job_py<4>(p, s, slice, lds, wave, ep); LOGT(s * 32 + 24 + slice, 2); }
    }
    LOGT(2048 + blockIdx.x, 2);
}
}

extern "C" void kernel_launch(void* const* d_in, const int* in_sizes, int n_in, void* d_out, int out_size, void* d_ws, size_t ws_size, hipStream_t stream) {
    Params p;
    p.x = (const u16*)d_in[0]; p.mem0 = (const u16*)d_in[1]; p.wq = (const u16*)d_in[2]; p.wk = (const u16*)d_in[3]; p.wv = (const u16*)d_in[4]; p.wo = (const u16*)d_in[5];
    p.wg = (const u16*)d_in[8]; p.wu = (const u16*)d_in[9]; p.wd = (const u16*)d_in[10]; p.c = (const u16*)d_in[11]; p.wmod = (const u16*)d_in[12];
    p.out = (u16*)d_out; p.ws = (unsigned char*)d_ws;
    (void)hipFuncSetAttribute((const void*)k_main, hipFuncAttributeMaxDynamicSharedMemorySize, L_END);
    hipLaunchKernelGGL(k_main, dim3(256), dim3(256), L_END, stream, p);
}
```
